```python
import torch

def _softmax(x, axis=-1):
    return torch.softmax(x, dim=axis)
def _gelu(x):
    return torch.nn.functional.gelu(x, approximate="tanh")
def _silu(x):
    return torch.nn.functional.silu(x)
def _sigmoid(x):
    return torch.sigmoid(x)
def _layernorm(x, axis=-1, eps=1e-5):
    m = x.mean(dim=axis, keepdim=True)
    v = x.var(dim=axis, keepdim=True, unbiased=False)
    return (x - m) / torch.sqrt(v + eps)
def _scatter_add(tgt, idx, src, axis=0):
    return tgt.clone().index_add_(axis, idx, src)
def _segment_sum(src, seg, n):
    out = torch.zeros((n,) + src.shape[1:], dtype=src.dtype)
    return out.index_add_(0, seg, src)
def _pad(x, pw):
    flat = tuple(v for pair in reversed(pw) for v in pair)
    return torch.nn.functional.pad(x, flat)

kernel_name = 'parallel_sum__ssm_conv_h3__swiglu_ffn__prompt'

def setup_inputs():

    _w = (lambda a: (lambda t: t.to(torch.bfloat16) if t.is_floating_point() else t)(torch.as_tensor(a)))
    def _A_setup():
        B, S, D = 1, 2048, 1024
        ks = 4
        return {
            "q": _w((lambda *s: torch.randn(*s, dtype=torch.bfloat16) * 0.05)(B, S, D)),
            "k": _w((lambda *s: torch.randn(*s, dtype=torch.bfloat16) * 0.8)(B, S, D)),
            "v": _w((lambda *s: torch.randn(*s, dtype=torch.bfloat16) * 0.8)(B, S, D)),
            "shift_k": _w((lambda *s: torch.randn(*s, dtype=torch.bfloat16) * 0.8)(D, ks) * 0.5),
            "ssm_k": _w((lambda *s: torch.randn(*s, dtype=torch.bfloat16) * 0.8)(D, S) * (1.0 / S ** 0.5)),
            "ssm_D": _w((lambda *s: torch.randn(*s, dtype=torch.bfloat16) * 0.8)(D)),
        }
    _A_in = _A_setup()

    def _B_setup():
        B, S, D, F = 1, 2048, 1024, 3584
        return {
            "x": _w((lambda *s: torch.randn(*s, dtype=torch.bfloat16) * 0.05)(B, S, D)),
            "w_gate": _w((lambda *s: torch.randn(*s, dtype=torch.bfloat16) * 0.05)(D, F)),
            "w_up": _w((lambda *s: torch.randn(*s, dtype=torch.bfloat16) * 0.05)(D, F)),
            "w_down": _w((lambda *s: torch.randn(*s, dtype=torch.bfloat16) * 0.05)(F, D)),
        }
    _B_in = _B_setup()

    return {
        "A_q": _A_in["q"],
        "A_k": _A_in["k"],
        "A_v": _A_in["v"],
        "A_shift_k": _A_in["shift_k"],
        "A_ssm_k": _A_in["ssm_k"],
        "A_ssm_D": _A_in["ssm_D"],
        "B_x": _B_in["x"],
        "B_w_gate": _B_in["w_gate"],
        "B_w_up": _B_in["w_up"],
        "B_w_down": _B_in["w_down"],
    }

def reference(A_q, A_k, A_v, A_shift_k, A_ssm_k, A_ssm_D, B_x, B_w_gate, B_w_up, B_w_down):
    def _A_ref(q, k, v, shift_k, ssm_k, ssm_D):
        B, S, D = q.shape
        ks = shift_k.shape[-1]
        k_pad = _pad(k, ((0, 0), (ks - 1, 0), (0, 0)))
        k_shift = torch.zeros_like(k)
        for t in range(ks):
            k_shift = k_shift + k_pad[:, t:t + S, :] * shift_k[:, ks - 1 - t][None, None, :]
        kv = k_shift * v
        L = 2 * S
        kv_pad = _pad(kv, ((0, 0), (0, S), (0, 0)))
        kf_pad = _pad(ssm_k, ((0, 0), (0, S)))
        kv_f = (lambda x, axes=(-2,-1): torch.fft.rfft2(x.float(), dim=axes))(kv_pad[:, None, :, :], axes=(1, 2))
        kf_f = (lambda x, axes=(-2,-1): torch.fft.rfft2(x.float(), dim=axes))(kf_pad[:, None, :], axes=(1, 2))
        kf_f = (lambda x, *d: x.permute(*d))(kf_f, 1, 2, 0)
        y_f = kv_f * kf_f[None, :, :, :]
        y = (lambda x, s, axes=(-2,-1): torch.fft.irfft2(x, s=s, dim=axes).to(torch.bfloat16))(y_f, (1, L), axes=(1, 2))[:, 0, :S, :]
        y = y + kv * ssm_D[None, None, :]
        return q * y

    def _B_ref(x, w_gate, w_up, w_down):
        g = _silu(x @ w_gate)
        u = x @ w_up
        return (g * u) @ w_down

    x = A_q
    xn = _layernorm(x, axis=-1)
    return x + _A_ref(xn, A_k, A_v, A_shift_k, A_ssm_k, A_ssm_D) + _B_ref(xn, B_w_gate, B_w_up, B_w_down)
```

```cpp
#include <hip/hip_runtime.h>
#include <hip/hip_bf16.h>

#define LAS __attribute__((address_space(3)))
#define DI __device__ __forceinline__
typedef unsigned short u16;
typedef short s16x4 __attribute__((ext_vector_type(4)));
typedef short bf16x8 __attribute__((ext_vector_type(8)));
typedef __bf16 bf2 __attribute__((ext_vector_type(2)));
typedef float f32x4 __attribute__((ext_vector_type(4)));
typedef unsigned u32x4 __attribute__((ext_vector_type(4)));
typedef unsigned u32x2 __attribute__((ext_vector_type(2)));

constexpr int S = 2048, D = 1024, F = 3584;

DI unsigned pk2(float lo, float hi) { bf2 p; p[0] = (__bf16)lo; p[1] = (__bf16)hi; return __builtin_bit_cast(unsigned, p); }
DI float lo16(unsigned v) { return __uint_as_float(v << 16); }
DI float hi16(unsigned v) { return __uint_as_float(v & 0xffff0000u); }
DI float rbf(float x) { return lo16(pk2(x, x)); }
#define WAIT_V(n) asm volatile("s_waitcnt vmcnt(" #n ")" ::: "memory")
#define WAIT_L(n) asm volatile("s_waitcnt lgkmcnt(" #n ")" ::: "memory")
#define BAR __builtin_amdgcn_s_barrier()
#define SCHED __builtin_amdgcn_sched_barrier(0)
#define GLDS(g, l) __builtin_amdgcn_global_load_lds((const unsigned*)(g), (LAS unsigned*)(l), 16, 0, 0)
#define MFMA16(a, b, c) __builtin_amdgcn_mfma_f32_16x16x32_bf16((a), (b), (c), 0, 0, 0)
#define TRRD(dst, base, off) asm volatile("ds_read_b64_tr_b16 %0, %1 offset:%2" : "=v"(dst) : "v"(base), "i"(off) : "memory")
struct TrFrag { s16x4 lo, hi; };
#define TRF(f, base, off) do { TRRD((f).lo, base, off); TRRD((f).hi, base, (off) + 1024); } while (0)
#define FRAG(f) __builtin_shufflevector((f).lo, (f).hi, 0, 1, 2, 3, 4, 5, 6, 7)

__global__ __launch_bounds__(256) void k_prep(const u16* __restrict__ q, const u16* __restrict__ k, const u16* __restrict__ v,
                                              const u16* __restrict__ shk, u16* __restrict__ xn, u16* __restrict__ kvTr) {
  __shared__ __attribute__((aligned(16))) unsigned char sm[64 * 272];
  const int tid = threadIdx.x, lane = tid & 63, wid = tid >> 6;
  if (blockIdx.x < 512) {
    const int row = blockIdx.x * 4 + wid;
    const u32x4 a = *(const u32x4*)(q + (size_t)row * D + lane * 8), b = *(const u32x4*)(q + (size_t)row * D + 512 + lane * 8);
    float x[16];
#pragma unroll
    for (int i = 0; i < 4; ++i) { x[2 * i] = lo16(a[i]); x[2 * i + 1] = hi16(a[i]); x[8 + 2 * i] = lo16(b[i]); x[9 + 2 * i] = hi16(b[i]); }
    float s = 0.f;
#pragma unroll
    for (int i = 0; i < 16; ++i) s += x[i];
#pragma unroll
    for (int o = 32; o >= 1; o >>= 1) s += __shfl_xor(s, o);
    const float mean = s * (1.0f / D);
    float q2 = 0.f;
#pragma unroll
    for (int i = 0; i < 16; ++i) { const float d = x[i] - mean; q2 += d * d; }
#pragma unroll
    for (int o = 32; o >= 1; o >>= 1) q2 += __shfl_xor(q2, o);
    const float mb = rbf(mean), vb = rbf(q2 * (1.0f / D)), sd = rbf(sqrtf(rbf(vb + 1e-5f)));
    u32x4 oa, ob;
#pragma unroll
    for (int i = 0; i < 4; ++i) {
      oa[i] = pk2(rbf(x[2 * i] - mb) / sd, rbf(x[2 * i + 1] - mb) / sd);
      ob[i] = pk2(rbf(x[8 + 2 * i] - mb) / sd, rbf(x[9 + 2 * i] - mb) / sd);
    }
    *(u32x4*)(xn + (size_t)row * D + lane * 8) = oa; *(u32x4*)(xn + (size_t)row * D + 512 + lane * 8) = ob;
  } else {
    const int b = blockIdx.x - 512, d0 = (b & 15) * 64, s0 = (b >> 4) * 128;
    const int dc = tid & 7, sg = tid >> 3, sb = s0 + 4 * sg, db = d0 + 8 * dc;
    u32x4 kr[7], vr[4], w[4];
#pragma unroll
    for (int i = 0; i < 7; ++i) { const int r = sb - 3 + i; kr[i] = r >= 0 ? *(const u32x4*)(k + (size_t)r * D + db) : (u32x4){0u, 0u, 0u, 0u}; }
#pragma unroll
    for (int i = 0; i < 4; ++i) vr[i] = *(const u32x4*)(v + (size_t)(sb + i) * D + db);
#pragma unroll
    for (int i = 0; i < 4; ++i) w[i] = *(const u32x4*)(shk + (size_t)db * 4 + 8 * i);
#pragma unroll
    for (int c = 0; c < 8; ++c) {
      const unsigned w01 = w[c >> 1][(c & 1) * 2], w23 = w[c >> 1][(c & 1) * 2 + 1];
      const float t0 = lo16(w01), t1 = hi16(w01), t2 = lo16(w23), t3 = hi16(w23);
      float kk[7], o[4];
#pragma unroll
      for (int i = 0; i < 7; ++i) kk[i] = (c & 1) ? hi16(kr[i][c >> 1]) : lo16(kr[i][c >> 1]);
#pragma unroll
      for (int i = 0; i < 4; ++i) {
        float a = rbf(kk[i] * t3);
        a = rbf(a + rbf(kk[i + 1] * t2)); a = rbf(a + rbf(kk[i + 2] * t1)); a = rbf(a + rbf(kk[i + 3] * t0));
        o[i] = a * ((c & 1) ? hi16(vr[i][c >> 1]) : lo16(vr[i][c >> 1]));
      }
      u32x2 p; p[0] = pk2(o[3], o[2]); p[1] = pk2(o[1], o[0]);
      *(u32x2*)(sm + (8 * dc + c) * 272 + (124 - 4 * sg) * 2) = p;
    }
    __syncthreads();
#pragma unroll
    for (int i = 0; i < 4; ++i) {
      const int idx = tid + 256 * i, dd = idx >> 4, ch = idx & 15;
      *(u32x4*)(kvTr + (size_t)(d0 + dd) * S + (S - 128 - s0) + ch * 8) = *(const u32x4*)(sm + dd * 272 + ch * 16);
    }
  }
}

constexpr int HTB = 16384;
__global__ __launch_bounds__(512, 2) void k_gemm1(const u16* __restrict__ A, const u16* __restrict__ Wg, const u16* __restrict__ Wu, u16* __restrict__ H) {
  extern __shared__ __attribute__((aligned(16))) unsigned char smem[];
  LAS unsigned char* lds = (LAS unsigned char*)smem;
  const int tid = threadIdx.x, wid = __builtin_amdgcn_readfirstlane(tid >> 6), lane = tid & 63, wr = wid >> 2, wc = wid & 3, fr = lane & 15, fq = lane >> 4;
  const int xg = blockIdx.x & 7, idx = blockIdx.x >> 3, pm = (xg & 1) * 4 + (idx & 3), pn = (xg >> 1) * 7 + (idx >> 2);
  unsigned voffA, voffB;
  { const int row = 8 * wid + (lane >> 3), ch = (lane & 7) ^ ((row >> 1) & 7); voffA = (unsigned)(row * D + ch * 8) * 2u; }
  { const int row = 4 * wid + (lane >> 4), f = (row & 1) | (((row >> 1) & 1) << 2) | (((row >> 3) & 1) << 3), ch = (lane & 15) ^ f; voffB = (unsigned)(row * F + ch * 8) * 2u; }
  const char* cA = (const char*)A + (size_t)pm * 256 * D * 2;
  const char* cG = (const char*)Wg + (size_t)pn * 256;
  const char* cU = (const char*)Wu + (size_t)pn * 256;
  const unsigned ldsw = (unsigned)wid * 1024u;
  const int aoff0 = (wr * 64 + fr) * 128 + ((fq ^ (fr >> 1)) << 4), aoff1 = aoff0 ^ 64;
  int boff0, boff1;
  { const int qq = fr >> 2, p = lane & 3, f = (qq & 1) | ((qq >> 1) << 2) | ((fq & 1) << 3);
    boff0 = (int)(unsigned)(size_t)lds + 4 * HTB + (8 * fq + qq) * 256 + (((4 * wc + p) ^ f) << 4) + ((p & 1) << 3); boff1 = boff0 ^ 8; }
#define SA(b, h) (((b) * 2 + (h)) * HTB)
#define SB(b, h) ((4 + (b) * 2 + (h)) * HTB)
#define STAGE_A(b, h, kt) do { const char* g_ = cA + (size_t)(h) * (128 * D * 2) + (size_t)(kt) * 128 + voffA; \
    GLDS(g_, lds + SA(b, h) + ldsw); GLDS(g_ + 64 * D * 2, lds + SA(b, h) + ldsw + 8192); } while (0)
#define STAGE_B(b, h, kt) do { const char* g_ = ((h) ? cU : cG) + (size_t)(kt) * (64 * F * 2) + voffB; \
    GLDS(g_, lds + SB(b, h) + ldsw); GLDS(g_ + 32 * F * 2, lds + SB(b, h) + ldsw + 8192); } while (0)
#define LDA(dst, b, h) do { _Pragma("unroll") for (int m = 0; m < 4; ++m) { \
    dst[m][0] = *(const LAS bf16x8*)(lds + SA(b, h) + aoff0 + m * 2048); dst[m][1] = *(const LAS bf16x8*)(lds + SA(b, h) + aoff1 + m * 2048); } } while (0)
#define LDB(dst, b, h) do { TRF(dst[0][0], boff0, ((b) * 2 + (h)) * HTB); TRF(dst[1][0], boff1, ((b) * 2 + (h)) * HTB); \
    TRF(dst[0][1], boff0, ((b) * 2 + (h)) * HTB + 8192); TRF(dst[1][1], boff1, ((b) * 2 + (h)) * HTB + 8192); } while (0)
#define WAIT_LB(Bt) asm volatile("s_waitcnt lgkmcnt(0)" : "+v"(Bt[0][0].lo), "+v"(Bt[0][0].hi), "+v"(Bt[0][1].lo), "+v"(Bt[0][1].hi), \
    "+v"(Bt[1][0].lo), "+v"(Bt[1][0].hi), "+v"(Bt[1][1].lo), "+v"(Bt[1][1].hi) :: "memory")
#define MMA(ai, bj, At, Bt) do { __builtin_amdgcn_s_setprio(1); _Pragma("unroll") for (int m = 0; m < 4; ++m) _Pragma("unroll") for (int n = 0; n < 2; ++n) \
    _Pragma("unroll") for (int kk = 0; kk < 2; ++kk) acc[ai][bj][m][n] = MFMA16(FRAG(Bt[n][kk]), At[m][kk], acc[ai][bj][m][n]); __builtin_amdgcn_s_setprio(0); } while (0)
  f32x4 acc[2][2][4][2];
#pragma unroll
  for (int a = 0; a < 2; ++a)
#pragma unroll
    for (int b = 0; b < 2; ++b)
#pragma unroll
      for (int m = 0; m < 4; ++m)
#pragma unroll
        for (int n = 0; n < 2; ++n) acc[a][b][m][n] = (f32x4){0.f, 0.f, 0.f, 0.f};
  bf16x8 At[4][2]; TrFrag B0[2][2], B1[2][2];
  constexpr int NT = D / 64;
  STAGE_B(0, 0, 0); STAGE_A(0, 0, 0); STAGE_B(0, 1, 0); STAGE_A(0, 1, 0);
  if (wr == 1) BAR;
  WAIT_V(4); BAR;
  STAGE_B(1, 0, 1); STAGE_A(1, 0, 1); STAGE_B(1, 1, 1);
  WAIT_V(6); BAR;
  for (int t = 0; t < NT; t += 2) {
    const int t1 = t + 1, t2 = (t + 2) & (NT - 1), t3 = (t + 3) & (NT - 1);
    LDB(B0, 0, 0); SCHED; LDA(At, 0, 0); STAGE_A(1, 1, t1);
    WAIT_L(8); BAR; WAIT_LB(B0); MMA(0, 0, At, B0); BAR; SCHED;
    LDB(B1, 0, 1); STAGE_B(0, 0, t2);
    BAR; WAIT_LB(B1); MMA(0, 1, At, B1); BAR;
    LDA(At, 0, 1); STAGE_A(0, 0, t2);
    BAR; WAIT_L(0); MMA(1, 0, At, B0); BAR; SCHED;
    STAGE_B(0, 1, t2);
    WAIT_V(6); BAR; MMA(1, 1, At, B1); BAR;
    LDB(B0, 1, 0); SCHED; LDA(At, 1, 0); STAGE_A(0, 1, t2);
    WAIT_L(8); BAR; WAIT_LB(B0); MMA(0, 0, At, B0); BAR; SCHED;
    LDB(B1, 1, 1); STAGE_B(1, 0, t3);
    BAR; WAIT_LB(B1); MMA(0, 1, At, B1); BAR;
    LDA(At, 1, 1); STAGE_A(1, 0, t3);
    BAR; WAIT_L(0); MMA(1, 0, At, B0); BAR; SCHED;
    STAGE_B(1, 1, t3);
    WAIT_V(6); BAR; MMA(1, 1, At, B1); BAR;
  }
  const bool odd = fq & 1;
#pragma unroll
  for (int ai = 0; ai < 2; ++ai)
#pragma unroll
    for (int m = 0; m < 4; ++m) {
      unsigned w[2][2];
#pragma unroll
      for (int n = 0; n < 2; ++n) {
        float h[4];
#pragma unroll
        for (int e = 0; e < 4; ++e) {
          const float g = rbf(acc[ai][0][m][n][e]), u = rbf(acc[ai][1][m][n][e]);
          const float sg = rbf(g * __builtin_amdgcn_rcpf(1.0f + __builtin_amdgcn_exp2f(g * -1.44269504089f)));
          h[e] = sg * u;
        }
        w[n][0] = pk2(h[0], h[1]); w[n][1] = pk2(h[2], h[3]);
      }
      u32x4 o; o[0] = odd ? w[1][0] : w[0][0]; o[1] = odd ? w[1][1] : w[0][1]; o[2] = odd ? w[0][0] : w[1][0]; o[3] = odd ? w[0][1] : w[1][1];
      const int mb = pm * 16 + ai * 8 + wr * 4 + m, kb = pn * 4 + wc;
      *(u32x4*)((char*)H + ((size_t)(mb * (F / 32) + kb) * 1024 + lane * 16)) = o;
    }
  WAIT_V(0);
  if (wr == 0) BAR;
#undef SA
#undef SB
#undef STAGE_A
#undef STAGE_B
#undef LDA
#undef LDB
#undef WAIT_LB
#undef MMA
}

constexpr int CV_CS = 4160, CV_U = 8 * CV_CS, CV_W = CV_U + 4608;
__global__ __launch_bounds__(256) void k_conv(const u16* __restrict__ hk, const u16* __restrict__ kvTr, const u16* __restrict__ sD, u16* __restrict__ y2T) {
  extern __shared__ __attribute__((aligned(16))) unsigned char smem[];
  const int tid = threadIdx.x, wid = __builtin_amdgcn_readfirstlane(tid >> 6), lane = tid & 63, fr = lane & 15, fq = lane >> 4;
  const int d = blockIdx.x * 4 + wid;
  LAS unsigned char* w = (LAS unsigned char*)smem + wid * CV_W;
  const u32x4* hrow = (const u32x4*)(hk + (size_t)d * S);
  const u32x4* urow = (const u32x4*)(kvTr + (size_t)d * S);
  const u32x4 z4 = {0u, 0u, 0u, 0u};
  u32x4 hl[5], hh[5], uu[4];
#pragma unroll
  for (int it = 0; it < 5; ++it) { const int cz = it * 64 + lane; hl[it] = (cz >= 2 && cz < 258) ? hrow[cz - 2] : z4; hh[it] = (cz >= 1 && cz < 257) ? hrow[cz - 1] : z4; }
#pragma unroll
  for (int it = 0; it < 4; ++it) uu[it] = urow[it * 64 + lane];
#pragma unroll
  for (int it = 0; it < 4; ++it) *(LAS u32x4*)(w + CV_U + (it * 64 + lane) * 16) = uu[it];
  if (lane < 32) *(LAS u32x4*)(w + CV_U + 4096 + lane * 16) = z4;
#pragma unroll
  for (int it = 0; it < 5; ++it) {
    const int cz = it * 64 + lane;
    if (it < 4 || lane < 4) {
      unsigned dd[8], sh[7];
#pragma unroll
      for (int i = 0; i < 4; ++i) { dd[i] = hl[it][i]; dd[4 + i] = hh[it][i]; }
#pragma unroll
      for (int i = 0; i < 7; ++i) sh[i] = __builtin_amdgcn_alignbit(dd[i + 1], dd[i], 16);
#pragma unroll
      for (int r = 0; r < 8; ++r) {
        u32x4 o;
#pragma unroll
        for (int i = 0; i < 4; ++i) o[i] = (r & 1) ? dd[(r + 1) / 2 + i] : sh[r / 2 + i];
        *(LAS u32x4*)(w + r * CV_CS + cz * 16) = o;
      }
    }
  }
  WAIT_L(0);
  f32x4 acc[8];
#pragma unroll
  for (int i = 0; i < 8; ++i) acc[i] = (f32x4){0.f, 0.f, 0.f, 0.f};
  const LAS unsigned char* pa = w + (fr & 7) * CV_CS + (fq + (fr >> 3)) * 16;
  const LAS unsigned char* pb = w + CV_U + (2032 - 16 * fr + 8 * fq) * 2 - 7 * 512;
#pragma unroll 2
  for (int m0 = 0; m0 < 8; ++m0) {
    bf16x8 a[8], b[8];
#pragma unroll
    for (int i = 0; i < 8; ++i) { b[i] = *(const LAS bf16x8*)(pb + (7 - i) * 512 + m0 * 64); a[i] = *(const LAS bf16x8*)(pa + i * 512 + m0 * 64); }
#pragma unroll
    for (int m1 = 0; m1 < 8; ++m1)
#pragma unroll
      for (int nt = m1; nt < 8; ++nt) acc[nt] = MFMA16(a[m1], b[nt - m1], acc[nt]);
  }
  const float Dd = lo16(sD[d]);
#pragma unroll
  for (int nt = 0; nt < 8; ++nt) {
    const int s0 = 256 * nt + 16 * fr + 4 * fq;
    const u32x2 ur = *(const LAS u32x2*)(w + CV_U + (2044 - s0) * 2);
    const float y0 = rbf(acc[nt][0]) + rbf(hi16(ur[1]) * Dd), y1 = rbf(acc[nt][1]) + rbf(lo16(ur[1]) * Dd);
    const float y2 = rbf(acc[nt][2]) + rbf(hi16(ur[0]) * Dd), y3 = rbf(acc[nt][3]) + rbf(lo16(ur[0]) * Dd);
    u32x2 o; o[0] = pk2(y0, y1); o[1] = pk2(y2, y3);
    *(u32x2*)(y2T + (size_t)d * S + s0) = o;
  }
}

constexpr int G2_NS = 6, G2_ST = 24576, G2_Y = G2_NS * G2_ST;
__global__ __launch_bounds__(512, 2) void k_gemm2(const u16* __restrict__ H, const u16* __restrict__ Wd, const u16* __restrict__ X, const u16* __restrict__ xn,
                                                  const u16* __restrict__ y2T, u16* __restrict__ out) {
  extern __shared__ __attribute__((aligned(16))) unsigned char smem[];
  LAS unsigned char* lds = (LAS unsigned char*)smem;
  const int tid = threadIdx.x, wid = __builtin_amdgcn_readfirstlane(tid >> 6), lane = tid & 63, wk = wid >> 2, wc = wid & 3, fr = lane & 15, fq = lane >> 4;
  const int xg = blockIdx.x & 7, idx = blockIdx.x >> 3, pm = (xg & 3) * 8 + (idx & 7), pn = (xg >> 2) * 4 + (idx >> 3);
  const char* gA = (const char*)H + ((size_t)((pm * 4 + (wid >> 1)) * (F / 32) + (wid & 1)) * 1024 + lane * 16);
  const char* gB;
  { const int row = 4 * wid + (lane >> 4), f = (row & 1) | (((row >> 1) & 1) << 2) | (((row >> 3) & 1) << 3), ch = (lane & 15) ^ f;
    gB = (const char*)Wd + (size_t)pn * 256 + (size_t)(row * D + ch * 8) * 2; }
  const unsigned ldsw = (unsigned)wid * 1024u;
#define STAGE(slot, kt) do { GLDS(gA + (size_t)(kt) * 2048, lds + (slot) * G2_ST + ldsw); \
    GLDS(gB + (size_t)(kt) * (64 * D * 2), lds + (slot) * G2_ST + 8192 + ldsw); GLDS(gB + (size_t)(kt) * (64 * D * 2) + 32 * D * 2, lds + (slot) * G2_ST + 16384 + ldsw); } while (0)
  const int aoff = wk * 1024 + lane * 16;
  int boff0, boff1;
  { const int qq = fr >> 2, p = lane & 3, f = (qq & 1) | ((qq >> 1) << 2) | ((fq & 1) << 3);
    boff0 = (int)(unsigned)(size_t)lds + 8192 + wk * 8192 + (8 * fq + qq) * 256 + (((4 * wc + p) ^ f) << 4) + ((p & 1) << 3); boff1 = boff0 ^ 8; }
#define LDF(Af, Bf, slot) do { if ((slot) < 2) { TRF(Bf[0], boff0, (slot) * G2_ST); TRF(Bf[1], boff1, (slot) * G2_ST); } \
    else if ((slot) < 4) { TRF(Bf[0], boff0 + 2 * G2_ST, ((slot) & 1) * G2_ST); TRF(Bf[1], boff1 + 2 * G2_ST, ((slot) & 1) * G2_ST); } \
    else { TRF(Bf[0], boff0 + 4 * G2_ST, ((slot) & 1) * G2_ST); TRF(Bf[1], boff1 + 4 * G2_ST, ((slot) & 1) * G2_ST); } \
    _Pragma("unroll") for (int m = 0; m < 4; ++m) Af[m] = *(const LAS bf16x8*)(lds + (slot) * G2_ST + aoff + m * 2048); } while (0)
#define MMA(Af, Bf) do { _Pragma("unroll") for (int m = 0; m < 4; ++m) _Pragma("unroll") for (int n = 0; n < 2; ++n) acc[m][n] = MFMA16(FRAG(Bf[n]), Af[m], acc[m][n]); } while (0)
#define WAIT_LB(Bf) asm volatile("s_waitcnt lgkmcnt(0)" : "+v"(Bf[0].lo), "+v"(Bf[0].hi), "+v"(Bf[1].lo), "+v"(Bf[1].hi) :: "memory")
  f32x4 acc[4][2];
#pragma unroll
  for (int m = 0; m < 4; ++m) { acc[m][0] = (f32x4){0.f, 0.f, 0.f, 0.f}; acc[m][1] = (f32x4){0.f, 0.f, 0.f, 0.f}; }
  constexpr int NT = F / 64;
  { const char* gy = (const char*)y2T + ((size_t)(pn * 128 + 8 * wid + (lane >> 3)) * S + pm * 64 + (lane & 7) * 8) * 2;
    GLDS(gy, lds + G2_Y + ldsw); GLDS(gy + (size_t)64 * S * 2, lds + G2_Y + 8192 + ldsw); }
#pragma unroll
  for (int i = 0; i < G2_NS; ++i) STAGE(i, i);
  bf16x8 A0[4], A1[4]; TrFrag B0[2], B1[2];
  WAIT_V(15); BAR;
  LDF(A0, B0, 0);
#define TRIP(slot, nslot, kt, Ac, Bc, An, Bn) do { WAIT_V(12); WAIT_LB(Bc); BAR; \
    { int kn_ = (kt) + G2_NS; kn_ = kn_ < NT ? kn_ : NT - 1; LDF(An, Bn, nslot); SCHED; STAGE(slot, kn_); } MMA(Ac, Bc); SCHED; } while (0)
  for (int t = 0; t < NT - 2; t += 6) {
    TRIP(0, 1, t, A0, B0, A1, B1); TRIP(1, 2, t + 1, A1, B1, A0, B0); TRIP(2, 3, t + 2, A0, B0, A1, B1);
    TRIP(3, 4, t + 3, A1, B1, A0, B0); TRIP(4, 5, t + 4, A0, B0, A1, B1); TRIP(5, 0, t + 5, A1, B1, A0, B0);
  }
  WAIT_V(12); WAIT_LB(B0); BAR;
  LDF(A1, B1, 1); MMA(A0, B0);
  WAIT_LB(B1); MMA(A1, B1);
  WAIT_V(0); WAIT_L(0); BAR;
  { LAS unsigned char* red = lds + wc * 8192 + lane * 16;
#pragma unroll
    for (int i = 0; i < 2; ++i)
#pragma unroll
      for (int n = 0; n < 2; ++n) *(LAS f32x4*)(red + wk * 4096 + (i * 2 + n) * 1024) = wk ? acc[i][n] : acc[2 + i][n];
    WAIT_L(0); BAR;
    const bool odd = fq & 1;
    const int qq = fr >> 2, p = lane & 3;
#pragma unroll
    for (int i = 0; i < 2; ++i) {
      f32x4 c[2];
#pragma unroll
      for (int n = 0; n < 2; ++n) c[n] = (wk ? acc[2 + i][n] : acc[i][n]) + *(const LAS f32x4*)(red + (wk ^ 1) * 4096 + (i * 2 + n) * 1024);
      const int m = wk * 2 + i;
      const size_t go = (size_t)(pm * 64 + m * 16 + fr) * D + pn * 128 + wc * 32 + fq * 8;
      const u32x4 xv = *(const u32x4*)(X + go), nv = *(const u32x4*)(xn + go);
      LAS unsigned char* yp = lds + G2_Y + (wc * 32 + fq * 8 + qq) * 128 + (m * 16 + 4 * p) * 2;
      const u32x2 y0 = __builtin_bit_cast(u32x2, __builtin_amdgcn_ds_read_tr16_b64_v4i16((LAS s16x4*)yp));
      const u32x2 y1 = __builtin_bit_cast(u32x2, __builtin_amdgcn_ds_read_tr16_b64_v4i16((LAS s16x4*)(yp + 512)));
      const unsigned yv[4] = {y0[0], y0[1], y1[0], y1[1]};
      const f32x4 cl = odd ? c[1] : c[0], ch = odd ? c[0] : c[1];
      const float bb[8] = {cl[0], cl[1], cl[2], cl[3], ch[0], ch[1], ch[2], ch[3]};
      u32x4 o;
#pragma unroll
      for (int j = 0; j < 4; ++j) {
        const float r0 = rbf(lo16(xv[j]) + rbf(lo16(nv[j]) * lo16(yv[j]))) + rbf(bb[2 * j]);
        const float r1 = rbf(hi16(xv[j]) + rbf(hi16(nv[j]) * hi16(yv[j]))) + rbf(bb[2 * j + 1]);
        o[j] = pk2(r0, r1);
      }
      *(u32x4*)(out + go) = o;
    }
  }
#undef STAGE
#undef LDF
#undef MMA
#undef WAIT_LB
#undef TRIP
}

#define STAGES 15
extern "C" void kernel_launch_stages(void* const* d_in, void* d_out, void* d_ws, hipStream_t stream, int stages) {
  const u16* q = (const u16*)d_in[0]; const u16* k = (const u16*)d_in[1]; const u16* v = (const u16*)d_in[2];
  const u16* shk = (const u16*)d_in[3]; const u16* hk = (const u16*)d_in[4]; const u16* sD = (const u16*)d_in[5];
  const u16* Wg = (const u16*)d_in[7]; const u16* Wu = (const u16*)d_in[8]; const u16* Wd = (const u16*)d_in[9];
  char* ws = (char*)d_ws;
  u16* xn = (u16*)ws; u16* kvTr = (u16*)(ws + (4 << 20)); u16* y2T = (u16*)(ws + (8 << 20)); u16* H = (u16*)(ws + (12 << 20));
  (void)hipFuncSetAttribute((const void*)k_gemm1, hipFuncAttributeMaxDynamicSharedMemorySize, 8 * HTB);
  (void)hipFuncSetAttribute((const void*)k_conv, hipFuncAttributeMaxDynamicSharedMemorySize, 4 * CV_W);
  (void)hipFuncSetAttribute((const void*)k_gemm2, hipFuncAttributeMaxDynamicSharedMemorySize, G2_Y + 16384);
  if (stages & 1) k_prep<<<768, 256, 0, stream>>>(q, k, v, shk, xn, kvTr);
  if (stages & 2) k_gemm1<<<224, 512, 8 * HTB, stream>>>(xn, Wg, Wu, H);
  if (stages & 4) k_conv<<<256, 256, 4 * CV_W, stream>>>(hk, kvTr, sD, y2T);
  if (stages & 8) k_gemm2<<<256, 512, G2_Y + 16384, stream>>>(H, Wd, q, xn, y2T, (u16*)d_out);
}

extern "C" void kernel_launch(void* const* d_in, const int* in_sizes, int n_in, void* d_out, int out_size, void* d_ws, size_t ws_size, hipStream_t stream) {
  kernel_launch_stages(d_in, d_out, d_ws, stream, 15);
}
```

```cpp
#include <hip/hip_runtime.h>
#include <hip/hip_bf16.h>

#define LAS __attribute__((address_space(3)))
#define DI __device__ __forceinline__
typedef unsigned short u16;
typedef short s16x4 __attribute__((ext_vector_type(4)));
typedef short bf16x8 __attribute__((ext_vector_type(8)));
typedef __bf16 bf2 __attribute__((ext_vector_type(2)));
typedef float f32x2 __attribute__((ext_vector_type(2)));
typedef float f32x4 __attribute__((ext_vector_type(4)));
typedef unsigned u32x4 __attribute__((ext_vector_type(4)));
typedef unsigned u32x2 __attribute__((ext_vector_type(2)));

constexpr int S = 2048, D = 1024, F = 3584;

template <int V> struct IC { static constexpr int value = V; constexpr operator int() const { return V; } };
template <int I, int N, class Fn> DI void static_for_(Fn&& f) { if constexpr (I < N) { f(IC<I>{}); static_for_<I + 1, N>(f); } }
template <int N, class Fn> DI void static_for(Fn&& f) { static_for_<0, N>(f); }
DI unsigned pk2(float lo, float hi) { bf2 p; p[0] = (__bf16)lo; p[1] = (__bf16)hi; return __builtin_bit_cast(unsigned, p); }
DI float lo16(unsigned v) { return __uint_as_float(v << 16); }
DI float hi16(unsigned v) { return __uint_as_float(v & 0xffff0000u); }
DI float rbf(float x) { return lo16(pk2(x, x)); }
DI float fma1(float a, float b, float c) { float r; asm("v_fma_f32 %0, %1, %2, %3" : "=v"(r) : "v"(a), "v"(b), "v"(c)); return r; }
DI float mul1(float a, float b) { float r; asm("v_mul_f32 %0, %1, %2" : "=v"(r) : "v"(a), "v"(b)); return r; }
DI float dot2(unsigned a, unsigned b, float c) { return __builtin_amdgcn_fdot2_f32_bf16(__builtin_bit_cast(bf2, a), __builtin_bit_cast(bf2, b), c, false); }
#define WAIT_V(n) asm volatile("s_waitcnt vmcnt(" #n ")" ::: "memory")
#define WAIT_L(n) asm volatile("s_waitcnt lgkmcnt(" #n ")" ::: "memory")
#define BAR __builtin_amdgcn_s_barrier()
#define SCHED __builtin_amdgcn_sched_barrier(0)
#define GLDS(g, l) __builtin_amdgcn_global_load_lds((const unsigned*)(g), (LAS unsigned*)(l), 16, 0, 0)
#define BLDS(rs, l, vo, so) __builtin_amdgcn_raw_ptr_buffer_load_lds((rs), (LAS void*)(l), 16, (vo), (so), 0, 0)
#define RSRC(p, bytes) __builtin_amdgcn_make_buffer_rsrc((void*)(p), 0, (bytes), 0x00020000)
#define MFMA16(a, b, c) __builtin_amdgcn_mfma_f32_16x16x32_bf16((a), (b), (c), 0, 0, 0)
template <int OFF> DI void rd128(bf16x8& dst, int base) { asm volatile("ds_read_b128 %0, %1 offset:%2" : "=v"(dst) : "v"(base), "i"(OFF) : "memory"); }
template <int OFF> DI void rd64(s16x4& dst, int base) { asm volatile("ds_read_b64 %0, %1 offset:%2" : "=v"(dst) : "v"(base), "i"(OFF) : "memory"); }
template <int OFF> DI void trrd(s16x4& dst, int base) { asm volatile("ds_read_b64_tr_b16 %0, %1 offset:%2" : "=v"(dst) : "v"(base), "i"(OFF) : "memory"); }
struct TrFrag { s16x4 lo, hi; };
template <int OFF> DI void trf(TrFrag& f, int base) { trrd<OFF>(f.lo, base); trrd<OFF + 1024>(f.hi, base); }
#define FRAG(f) __builtin_shufflevector((f).lo, (f).hi, 0, 1, 2, 3, 4, 5, 6, 7)

#define STAMP_DECL
#define ST_PARAM
#define ST_ARG
#define STAMP(i)
#define STAMP_OUT(kid, bid)

#define SIDE_DELAY 32
typedef unsigned long long u64;
constexpr int CT_K = 64, CT_Y = 128, CT_S = 384, CT_H = CT_S + 2048, CT_X = CT_H + 256;
DI int opaque_lane() { int t = threadIdx.x; asm volatile("" : "+v"(t)); return t & 63; }
DI void ld_flag_async(u64& dst, const u64* p) { asm volatile("global_load_dwordx2 %0, %1, off sc1" : "=v"(dst) : "v"(p) : "memory"); }
DI void pin(u64& x) { asm volatile("" : "+v"(x)); }
DI u64 uniform64(u64 x) { return ((u64)__builtin_amdgcn_readfirstlane((unsigned)(x >> 32)) << 32) | __builtin_amdgcn_readfirstlane((unsigned)x); }
DI u64 ld_agent(const u64* p) { return __hip_atomic_load(p, __ATOMIC_RELAXED, __HIP_MEMORY_SCOPE_AGENT); }
DI void st_agent(u64* p, u64 x) { __hip_atomic_store(p, x, __ATOMIC_RELAXED, __HIP_MEMORY_SCOPE_AGENT); }
template <int N> DI void poll_flags(const u64* p, u64 tag, int last = 0) {
  for (int spin = 0; spin < (1 << 22); ++spin) {
    bool ok = true;
#pragma unroll
    for (int i = 0; i < N; ++i) ok &= ld_agent(p + 64 * i + (i == N - 1 ? last : 0)) == tag;
    if (__all(ok)) break;
    __builtin_amdgcn_s_sleep(2);
  }
}
template <int CTRL> DI float dpp_add(float x) { return x + __uint_as_float(__builtin_amdgcn_update_dpp(0u, __float_as_uint(x), CTRL, 0xf, 0xf, true)); }
DI float wave_sum(float x) {
  x = dpp_add<0xb1>(x); x = dpp_add<0x4e>(x); x = dpp_add<0x141>(x); x = dpp_add<0x140>(x);
  const unsigned u = __float_as_uint(x);
  return (__uint_as_float(__builtin_amdgcn_readlane(u, 0)) + __uint_as_float(__builtin_amdgcn_readlane(u, 16))) +
         (__uint_as_float(__builtin_amdgcn_readlane(u, 32)) + __uint_as_float(__builtin_amdgcn_readlane(u, 48)));
}
DI void side_prep(LAS unsigned char* lds, int hb, u64 tag, u64* ctl, const u16* __restrict__ X, const u16* __restrict__ k, const u16* __restrict__ v,
                  const u16* __restrict__ shk, u16* __restrict__ kvT ST_PARAM) {
  const int tid = threadIdx.x, wid = __builtin_amdgcn_readfirstlane(tid >> 6), lane = tid & 63;
  const int cc = tid & 15, rg = tid >> 4, s0 = (hb >> 3) * 512 + 16 * rg, c0 = (hb & 7) * 128 + 8 * cc;
  const int vo = (s0 * D + c0) * 2, vh = s0 ? vo - 3 * D * 2 : 1 << 30;
  const auto rk = RSRC(k, S * D * 2), rv = RSRC(v, S * D * 2), rx = RSRC(X, S * D * 2);
  u32x4 xr[8][2], kr[19], vr[16], wv[4];
#pragma unroll
  for (int i = 0; i < 8; ++i) { xr[i][0] = __builtin_amdgcn_raw_buffer_load_b128(rx, (hb * 64 + wid * 8) * D * 2 + lane * 16, i * D * 2, 0);
                                xr[i][1] = __builtin_amdgcn_raw_buffer_load_b128(rx, (hb * 64 + wid * 8) * D * 2 + 1024 + lane * 16, i * D * 2, 0); }
#pragma unroll
  for (int i = 0; i < 4; ++i) wv[i] = *(const u32x4*)(shk + (size_t)c0 * 4 + 8 * i);
#pragma unroll
  for (int i = 0; i < 3; ++i) kr[i] = __builtin_amdgcn_raw_buffer_load_b128(rk, vh, i * D * 2, 0);
#pragma unroll
  for (int i = 0; i < 16; ++i) { kr[3 + i] = __builtin_amdgcn_raw_buffer_load_b128(rk, vo, i * D * 2, 0); vr[i] = __builtin_amdgcn_raw_buffer_load_b128(rv, vo, i * D * 2, 0); }
  SCHED;
  STAMP(1);
#pragma unroll
  for (int r = 0; r < 8; ++r) {
    f32x2 x[8];
#pragma unroll
    for (int i = 0; i < 8; ++i) x[i] = (f32x2){lo16(xr[r][i >> 2][i & 3]), hi16(xr[r][i >> 2][i & 3])};
    const f32x2 s = ((x[0] + x[1]) + (x[2] + x[3])) + ((x[4] + x[5]) + (x[6] + x[7]));
    const float mean = wave_sum(s[0] + s[1]) * (1.0f / D);
    f32x2 q = {0.f, 0.f};
#pragma unroll
    for (int i = 0; i < 8; ++i) { const f32x2 d = x[i] - mean; q += d * d; }
    const float sd = __builtin_amdgcn_sqrtf(rbf(rbf(wave_sum(q[0] + q[1]) * (1.0f / D)) + 1e-5f));
    if (lane == 0) st_agent(ctl + CT_S + hb * 64 + wid * 8 + r, (tag << 32) | pk2(mean, sd));
    SCHED;
  }
  STAMP(2);
#pragma unroll
  for (int j = 0; j < 4; ++j) {
    const f32x2 t0 = {lo16(wv[j][0]), lo16(wv[j][2])}, t1 = {hi16(wv[j][0]), hi16(wv[j][2])}, t2 = {lo16(wv[j][1]), lo16(wv[j][3])}, t3 = {hi16(wv[j][1]), hi16(wv[j][3])};
    f32x2 kf[19], a[16];
#pragma unroll
    for (int i = 0; i < 19; ++i) kf[i] = (f32x2){lo16(kr[i][j]), hi16(kr[i][j])};
#pragma unroll
    for (int i = 0; i < 16; ++i) a[i] = (kf[i] * t3 + kf[i + 1] * t2 + kf[i + 2] * t1 + kf[i + 3] * t0) * (f32x2){lo16(vr[i][j]), hi16(vr[i][j])};
#pragma unroll
    for (int e = 0; e < 2; ++e)
#pragma unroll
      for (int h = 0; h < 2; ++h) {
        const int t = 15 - 8 * h;
        *(LAS u32x4*)(lds + (8 * cc + 2 * j + e) * 1024 + (((62 - 2 * rg + h) ^ (cc & 7)) << 4)) =
            (u32x4){pk2(a[t][e], a[t - 1][e]), pk2(a[t - 2][e], a[t - 3][e]), pk2(a[t - 4][e], a[t - 5][e]), pk2(a[t - 6][e], a[t - 7][e])};
      }
    SCHED;
  }
  STAMP(3);
  WAIT_L(0); BAR;
  asm volatile("" ::: "memory");
  const auto rt = RSRC(kvT, S * D * 2);
#pragma unroll
  for (int i = 0; i < 16; ++i) {
    const int ch = wid * 16 + i;
    const u32x4 o = *(const LAS u32x4*)(lds + ch * 1024 + ((lane ^ ((ch >> 3) & 7)) << 4));
    __builtin_amdgcn_raw_buffer_store_b128(o, rt, (((hb & 7) * 128 + ch) * S + (S - 512 - (hb >> 3) * 512) + lane * 8) * 2, 0, 16);
  }
  WAIT_V(0); BAR;
  if (tid == 0) st_agent(ctl + CT_K + (hb & 7) * 4 + (hb >> 3), tag);
  STAMP(4);
}

constexpr int CV_CP = 4160, CV_TB = 4 * CV_CP, CV_RAW = 4 * CV_TB, CV_U = CV_RAW + 8 * 3072, CV_UB = 4608;
DI void conv_fetch(LAS unsigned char* lds, int c0, const u16* __restrict__ hk, const u16* kvT, int wid, int lane) {
  const auto rh = RSRC(hk + (size_t)(c0 + (wid & 3)) * S, S * 2), ru = RSRC(kvT, S * D * 2);
#pragma unroll
  for (int i = 0; i < 3; ++i) BLDS(rh, lds + CV_RAW + wid * 3072 + i * 1024, ((wid >> 2) * 128 + i * 64 + lane - 2) * 16, 0);
#pragma unroll
  for (int i = 0; i < 2; ++i)
    __builtin_amdgcn_raw_ptr_buffer_load_lds(ru, (LAS void*)(lds + CV_U + (wid & 3) * CV_UB + ((wid >> 2) * 2 + i) * 1024), 16, lane * 16,
                                             (c0 + (wid & 3)) * S * 2 + ((wid >> 2) * 2 + i) * 1024, 0, 16);
}
struct CvSet { TrFrag a[8]; bf16x8 b[8]; };
template <int M0, int J> DI void cv_read(CvSet& Z, int pa, int pb) {
  if constexpr (J < 8) rd128<(7 - J) * 512 + M0 * 64>(Z.b[J], pb);
  else { constexpr int H = J - 8; rd64<(H >> 1) * 512 + M0 * 64 + (H & 1) * 8>((H & 1) ? Z.a[H >> 1].hi : Z.a[H >> 1].lo, pa); }
}
template <int M0, class Fill> DI void cv_step(f32x4 (&acc)[8], CvSet& C, CvSet& N, int pa, int pb, const bf16x8& dg, Fill& fill) {
  asm volatile("s_waitcnt lgkmcnt(0)" : "+v"(C.a[0].lo), "+v"(C.a[0].hi), "+v"(C.a[1].lo), "+v"(C.a[1].hi), "+v"(C.a[2].lo), "+v"(C.a[2].hi), "+v"(C.a[3].lo), "+v"(C.a[3].hi),
               "+v"(C.a[4].lo), "+v"(C.a[4].hi), "+v"(C.a[5].lo), "+v"(C.a[5].hi), "+v"(C.a[6].lo), "+v"(C.a[6].hi), "+v"(C.a[7].lo), "+v"(C.a[7].hi),
               "+v"(C.b[0]), "+v"(C.b[1]), "+v"(C.b[2]), "+v"(C.b[3]), "+v"(C.b[4]), "+v"(C.b[5]), "+v"(C.b[6]), "+v"(C.b[7]) :: "memory");
  if constexpr (M0 == 0) static_for<8>([&](auto T_) { acc[T_] = MFMA16(dg, C.b[T_], ((f32x4){0.f, 0.f, 0.f, 0.f})); });
  static_for<8>([&](auto M1_) { constexpr int M1 = M1_;
    static_for<8 - M1>([&](auto T_) { constexpr int T = T_, I = M1 * 8 - M1 * (M1 - 1) / 2 + T;
      acc[M1 + T] = MFMA16(FRAG(C.a[M1]), C.b[T], acc[M1 + T]);
      if constexpr (M0 < 3 && I < 24) cv_read<M0 + 1, I>(N, pa, pb);
      if constexpr (M0 == 1 && I == 28) fill();
      SCHED; }); });
}
template <class Fill> DI void conv_run(LAS unsigned char* lds, int c0, unsigned dw, u16* y2T, int wid, int lane, Fill&& fill) {
  const int fr = lane & 15, fq = lane >> 4, d = c0 + (wid & 3), hf = wid >> 2;
  LAS unsigned char* tb = lds + (wid & 3) * CV_TB;
  LAS unsigned char* raw = lds + CV_RAW + wid * 3072;
  LAS unsigned char* ub = lds + CV_U + (wid & 3) * CV_UB;
#define CYC(i)
  u32x4 rl[3]; u32x2 rh[3];
#pragma unroll
  for (int it = 0; it < 3; ++it) { rl[it] = *(const LAS u32x4*)(raw + (64 * it + (it < 2 ? lane : lane & 1)) * 16); rh[it] = *(const LAS u32x2*)(raw + (64 * it + (it < 2 ? lane : lane & 1)) * 16 + 16); }
  if (lane < 16) *(LAS u32x4*)(ub + 4096 + hf * 256 + lane * 16) = (u32x4){0u, 0u, 0u, 0u};
#pragma unroll
  for (int it = 0; it < 3; ++it) {
    const unsigned dd[6] = {rl[it][0], rl[it][1], rl[it][2], rl[it][3], rh[it][0], rh[it][1]};
    unsigned sh[5];
#pragma unroll
    for (int i = 0; i < 5; ++i) sh[i] = __builtin_amdgcn_alignbit(dd[i + 1], dd[i], 16);
    LAS unsigned char* p = tb + (128 * hf + 64 * it + (it < 2 ? lane : lane & 1)) * 16;
    *(LAS u32x4*)(p) = (u32x4){sh[0], sh[1], sh[2], sh[3]};
    *(LAS u32x4*)(p + CV_CP) = (u32x4){dd[1], dd[2], dd[3], dd[4]};
    *(LAS u32x4*)(p + 2 * CV_CP) = (u32x4){sh[1], sh[2], sh[3], sh[4]};
    *(LAS u32x4*)(p + 3 * CV_CP) = (u32x4){dd[2], dd[3], dd[4], dd[5]};
  }
  const int pa = (int)(unsigned)(size_t)tb + (fr & 3) * CV_CP + ((fr >> 2) * 4 + 8 * fq) * 2 + hf * 256;
  const int pb = (int)(unsigned)(size_t)ub + (2032 - 16 * fr + 8 * fq) * 2 - 7 * 512 + hf * 256;
  const int dj = hf ? -2 : 15 - fr - 8 * fq;
  const unsigned db = ((d & 1) ? dw >> 16 : dw & 0xffffu) << ((dj & 1) * 16);
  const u32x4 dgw = {(dj >> 1) == 0 ? db : 0u, (dj >> 1) == 1 ? db : 0u, (dj >> 1) == 2 ? db : 0u, (dj >> 1) == 3 ? db : 0u};
  const bf16x8 dg = __builtin_bit_cast(bf16x8, dgw);
  WAIT_L(0); CYC(0); BAR;
  CYC(1);
  f32x4 acc[8];
  { CvSet X, Y;
    static_for<24>([&](auto J_) { cv_read<0, J_>(X, pa, pb); });
    cv_step<0>(acc, X, Y, pa, pb, dg, fill); cv_step<1>(acc, Y, X, pa, pb, dg, fill); cv_step<2>(acc, X, Y, pa, pb, dg, fill); cv_step<3>(acc, Y, X, pa, pb, dg, fill); }
  LAS f32x4* xo = (LAS f32x4*)(lds + (wid < 6 ? CV_RAW + wid * 4096 : 112 * 1024 + wid * 4096)) + lane;
  LAS f32x4* xi = (LAS f32x4*)(lds + ((wid ^ 4) < 6 ? CV_RAW + (wid ^ 4) * 4096 : 112 * 1024 + (wid ^ 4) * 4096)) + lane;
#pragma unroll
  for (int i = 0; i < 4; ++i) xo[i * 64] = hf ? acc[i] : acc[4 + i];
  WAIT_L(0); CYC(2); BAR;
  CYC(3);
  asm volatile("" ::: "memory");
#pragma unroll
  for (int i = 0; i < 4; ++i) {
    const f32x4 a = (hf ? acc[4 + i] : acc[i]) + xi[i * 64];
    __builtin_amdgcn_raw_buffer_store_b64((u32x2){pk2(a[0], a[1]), pk2(a[2], a[3])}, RSRC(y2T, S * D * 2), d * S * 2 + (16 * fr + 4 * fq) * 2 + hf * 2048 + i * 512, 0, 16);
  }
  CYC(4);
}

constexpr int HTB = 16384, G1_CS = 8 * HTB, G1_SG = G1_CS + 2048, G1_CNT = G1_SG + 3072, LDS_BYTES = 160 * 1024;
constexpr int G2_ST = 32768;
__global__ __launch_bounds__(512, 2) void k_l1(const u16* __restrict__ X, const u16* __restrict__ Wg, const u16* __restrict__ Wu, u16* H, u64* ctl,
                                               const u16* __restrict__ k, const u16* __restrict__ v, const u16* __restrict__ shk, const u16* __restrict__ hk,
                                               const u16* __restrict__ sD, const u16* __restrict__ Wd, u16* kvT, u16* y2T, float* xbuf, u16* __restrict__ out) {
  extern __shared__ __attribute__((aligned(16))) unsigned char smem[];
  LAS unsigned char* lds = (LAS unsigned char*)smem;
  STAMP_DECL; STAMP(0);
  const int tid = threadIdx.x, wid = __builtin_amdgcn_readfirstlane(tid >> 6), lane = tid & 63, wr = wid >> 2, wc = wid & 3, fr = lane & 15, fq = lane >> 4;
  const u64 epoch = ld_agent(ctl);
  const unsigned dw = ((const __attribute__((address_space(4))) unsigned*)sD)[blockIdx.x * 2 + (wc >> 1)];
  const int lb = (int)(unsigned)(size_t)lds;
  LAS unsigned char* ldw = lds + wid * 1024;
  const auto rH = RSRC(H, S * F * 2);
  u64 tag;
  int hflag;
  const int kh = blockIdx.x & 1, pm2 = ((blockIdx.x >> 1) & 3) * 4 + ((blockIdx.x >> 3) & 3), pn2 = blockIdx.x >> 5;
  int voffW;
  auto set_voffW = [&] { const int ln = opaque_lane(), row = 4 * wid + (ln >> 4), f = (row & 1) | (((row >> 1) & 1) << 2) | (((row >> 3) & 1) << 3), ch = (ln & 15) ^ f;
    voffW = (kh * (F / 2) + row) * D * 2 + pn2 * 256 + ch * 16; };
  const auto rW = RSRC(Wd, F * D * 2);
#define STG_B(j) do { BLDS(rW, ldw + (((j) + 3) % 5) * G2_ST + 16384, voffW, (j) * (64 * D * 2)); BLDS(rW, ldw + (((j) + 3) % 5) * G2_ST + 24576, voffW, (j) * (64 * D * 2) + 32 * D * 2); } while (0)
  if (tid == 0) *(LAS unsigned*)(lds + G1_CNT) = 0u;
  if (blockIdx.x < 32) {
    tag = uniform64(epoch) + 1;
    __builtin_amdgcn_s_sleep(SIDE_DELAY);
    side_prep(lds, blockIdx.x, tag, ctl, X, k, v, shk, kvT ST_ARG);
    poll_flags<1>(ctl + CT_K + (lane & 3), tag);
    conv_fetch(lds, blockIdx.x * 4, hk, kvT, wid, lane);
#pragma unroll
    for (int i = 0; i < 4; ++i) { int ln = (blockIdx.x * 8 + wid) * 224 + i * 64 + lane; ln = (i == 3 && lane >= 32) ? ln - 64 : ln;
      __builtin_amdgcn_global_load_lds((const unsigned*)Wd + (size_t)ln * 32, (LAS unsigned*)(lds + G1_CNT + 256), 4, 0, 0); }
    WAIT_V(0);
    set_voffW(); STG_B(0); STG_B(1);
    hflag = (blockIdx.x & 7) * 32 + 28 + (blockIdx.x >> 3);
  } else {
  const int xg = blockIdx.x & 7, idx = (blockIdx.x >> 3) - 4, pm = (xg & 1) * 4 + (idx & 3), pn = (xg >> 1) * 7 + (idx >> 2);
  const int krot = (xg & 1) * 8;
  int voffA, voffB;
  { const int row = 8 * wid + (lane >> 3), ch = (lane & 7) ^ ((row >> 1) & 7); voffA = (pm * 256 + row) * D * 2 + ch * 16; }
  { const int row = 4 * wid + (lane >> 4), f = (row & 1) | (((row >> 1) & 1) << 2) | (((row >> 3) & 1) << 3), ch = (lane & 15) ^ f; voffB = row * F * 2 + pn * 256 + ch * 16; }
  const auto rA = RSRC(X, S * D * 2), rG = RSRC(Wg, D * F * 2), rU = RSRC(Wu, D * F * 2);
  const int aoff0 = lb + (wr * 64 + fr) * 128 + (((fq + 4 * wr) ^ (fr >> 1)) << 4), aoff1 = aoff0 ^ 64;
  int boff[2][2];
  { const int qq = fr >> 2, p = lane & 3, f = (qq & 1) | ((qq >> 1) << 2) | ((fq & 1) << 3);
    boff[0][0] = lb + 4 * HTB + wr * 8192 + (8 * fq + qq) * 256 + (((4 * wc + p) ^ f) << 4) + ((p & 1) << 3);
    boff[1][0] = boff[0][0] ^ 8; boff[0][1] = boff[0][0] ^ 8192; boff[1][1] = boff[1][0] ^ 8192; }
#define STAGE_A(b, h, kt) do { BLDS(rA, ldw + ((b) * 2 + (h)) * HTB, voffA, (h) * (128 * D * 2) + ((kt) ^ krot) * 128); \
    BLDS(rA, ldw + ((b) * 2 + (h)) * HTB + 8192, voffA, (h) * (128 * D * 2) + 64 * D * 2 + ((kt) ^ krot) * 128); } while (0)
#define STAGE_B(b, h, kt) do { BLDS((h) ? rU : rG, ldw + (4 + (b) * 2 + (h)) * HTB, voffB, ((kt) ^ krot) * (64 * F * 2)); \
    BLDS((h) ? rU : rG, ldw + (4 + (b) * 2 + (h)) * HTB + 8192, voffB, ((kt) ^ krot) * (64 * F * 2) + 32 * F * 2); } while (0)
#define LDA(b, h) do { static_for<4>([&](auto M_) { rd128<((b) * 2 + (h)) * HTB + M_ * 2048>(At[M_][0], aoff0); rd128<((b) * 2 + (h)) * HTB + M_ * 2048>(At[M_][1], aoff1); }); } while (0)
#define LDB(dst, b, h) do { trf<((b) * 2 + (h)) * HTB>(dst[0][0], boff[0][0]); trf<((b) * 2 + (h)) * HTB>(dst[1][0], boff[1][0]); \
    trf<((b) * 2 + (h)) * HTB>(dst[0][1], boff[0][1]); trf<((b) * 2 + (h)) * HTB>(dst[1][1], boff[1][1]); } while (0)
#define OPA "+v"(At[0][0]), "+v"(At[0][1]), "+v"(At[1][0]), "+v"(At[1][1]), "+v"(At[2][0]), "+v"(At[2][1]), "+v"(At[3][0]), "+v"(At[3][1])
#define OPB(Bt) "+v"(Bt[0][0].lo), "+v"(Bt[0][0].hi), "+v"(Bt[0][1].lo), "+v"(Bt[0][1].hi), "+v"(Bt[1][0].lo), "+v"(Bt[1][0].hi), "+v"(Bt[1][1].lo), "+v"(Bt[1][1].hi)
#define WAIT_A() asm volatile("s_waitcnt lgkmcnt(0)" : OPA :: "memory")
#define WAIT_B(Bt) asm volatile("s_waitcnt lgkmcnt(0)" : OPB(Bt) :: "memory")
#define WAIT_AB(Bt) asm volatile("s_waitcnt lgkmcnt(0)" : OPA, OPB(Bt) :: "memory")
#define MMA(ai, bj, Bt) do { __builtin_amdgcn_s_setprio(1); _Pragma("unroll") for (int kk = 0; kk < 2; ++kk) _Pragma("unroll") for (int m = 0; m < 4; ++m) _Pragma("unroll") for (int n = 0; n < 2; ++n) \
    acc[ai][bj][m][n] = MFMA16(FRAG(Bt[n][kk]), At[m][kk], acc[ai][bj][m][n]); \
    if (ai) { cs[bj][0] = MFMA16(FRAG(Bt[0][0]), ones, cs[bj][0]); cs[bj][1] = MFMA16(FRAG(Bt[1][0]), ones, cs[bj][1]); } __builtin_amdgcn_s_setprio(0); } while (0)
  STAGE_B(0, 0, 0); STAGE_A(0, 0, 0); STAGE_B(0, 1, 0); STAGE_A(0, 1, 0); STAGE_B(1, 0, 1); STAGE_A(1, 0, 1); STAGE_B(1, 1, 1);
  f32x4 acc[2][2][4][2], cs[2][2];
#pragma unroll
  for (int a = 0; a < 2; ++a)
#pragma unroll
    for (int b = 0; b < 2; ++b) {
      cs[a][b] = (f32x4){0.f, 0.f, 0.f, 0.f};
#pragma unroll
      for (int m = 0; m < 4; ++m)
#pragma unroll
        for (int n = 0; n < 2; ++n) acc[a][b][m][n] = (f32x4){0.f, 0.f, 0.f, 0.f};
    }
  bf16x8 ones = {0x3f80, 0x3f80, 0x3f80, 0x3f80, 0x3f80, 0x3f80, 0x3f80, 0x3f80};
  asm volatile("" : "+v"(ones));
  bf16x8 At[4][2]; TrFrag B0[2][2], B1[2][2];
  constexpr int NT = D / 64;
  WAIT_V(6);
  tag = uniform64(epoch) + 1;
  BAR;
  if (wr == 1) BAR;
  STAMP(6);
#define KTILE(b, P1, P2, P3, P4, W4) do { \
    LDB(B0, b, 0); LDA(b, 0); P1; SCHED; WAIT_L(8); BAR; WAIT_AB(B0); MMA(0, 0, B0); BAR; SCHED; \
    LDB(B1, b, 1); P2; SCHED; BAR; WAIT_B(B1); MMA(0, 1, B1); BAR; SCHED; \
    LDA(b, 1); P3; SCHED; BAR; WAIT_A(); MMA(1, 0, B0); BAR; SCHED; \
    P4; SCHED; W4; BAR; MMA(1, 1, B1); BAR; SCHED; } while (0)
  for (int t = 0; t < NT - 2; t += 2) {
    KTILE(0, STAGE_A(1, 1, t + 1), STAGE_B(0, 0, t + 2), STAGE_A(0, 0, t + 2), STAGE_B(0, 1, t + 2), WAIT_V(6));
    KTILE(1, STAGE_A(0, 1, t + 2), STAGE_B(1, 0, t + 3), STAGE_A(1, 0, t + 3), STAGE_B(1, 1, t + 3), WAIT_V(6));
  }
#define STAGE_S() do { if (wid < 3) __builtin_amdgcn_raw_ptr_buffer_load_lds(RSRC(ctl, CT_H * 8), (LAS void*)(ldw + G1_SG), 16, lane * 16, \
    wid < 2 ? CT_S * 8 + pm * 2048 + wid * 1024 : CT_K * 8, 0, 16); } while (0)
  KTILE(0, STAGE_A(1, 1, NT - 1), STAGE_S(), (void)0, (void)0, WAIT_V(0));
  KTILE(1, (void)0, (void)0, (void)0, (void)0, (void)0);
  STAMP(7);
  LAS f32x4* cst = (LAS f32x4*)(lds + G1_CS);
  if (fr == 0) {
#pragma unroll
    for (int i = 0; i < 4; ++i) cst[(wid * 4 + i) * 4 + fq] = cs[i >> 1][i & 1];
  }
  WAIT_L(0);
  if (wr == 0) BAR;
  BAR;
  asm volatile("" ::: "memory");
  if (!__all(*(const LAS u64*)(lds + G1_SG + 2048 + ((blockIdx.x >> 5) * 4 + (lane & 3)) * 8) == tag)) poll_flags<1>(ctl + CT_K + (blockIdx.x >> 5) * 4 + (lane & 3), tag);
  conv_fetch(lds, blockIdx.x * 4, hk, kvT, wid, opaque_lane());
  set_voffW(); STG_B(0); STG_B(1);
#pragma unroll
  for (int i = 0; i < 4; ++i) cs[i >> 1][i & 1] += cst[((wid ^ 4) * 4 + i) * 4 + fq];
  unsigned sg[8];
  { bool ok = true;
#pragma unroll
    for (int i = 0; i < 8; ++i) { const u32x2 g = *(const LAS u32x2*)(lds + G1_SG + ((i >> 2) * 128 + wr * 64 + (i & 3) * 16 + fr) * 8); sg[i] = g[0]; ok &= g[1] == (unsigned)tag; }
    if (__builtin_expect(!__all(ok), 0)) {
      for (int spin = 0; spin < (1 << 22); ++spin) {
        ok = true;
#pragma unroll
        for (int i = 0; i < 8; ++i) { const u64 g = ld_agent(ctl + CT_S + pm * 256 + (i >> 2) * 128 + wr * 64 + (i & 3) * 16 + fr); sg[i] = (unsigned)g; ok &= (unsigned)(g >> 32) == (unsigned)tag; }
        if (__all(ok)) break;
      }
    } }
#pragma unroll
  for (int i = 0; i < 8; ++i) {
    const float mu = lo16(sg[i]), c1 = __builtin_amdgcn_rcpf(hi16(sg[i])) * -1.44269504089f;
    u32x4 o;
#pragma unroll
    for (int n = 0; n < 2; ++n) {
      const f32x4 g = acc[i >> 2][0][i & 3][n] - cs[0][n] * mu, u = acc[i >> 2][1][i & 3][n] - cs[1][n] * mu, e = g * c1;
      f32x4 t = {__builtin_amdgcn_exp2f(e[0]), __builtin_amdgcn_exp2f(e[1]), __builtin_amdgcn_exp2f(e[2]), __builtin_amdgcn_exp2f(e[3])};
      t += 1.0f;
      const f32x4 h = g * u * (f32x4){__builtin_amdgcn_rcpf(t[0]), __builtin_amdgcn_rcpf(t[1]), __builtin_amdgcn_rcpf(t[2]), __builtin_amdgcn_rcpf(t[3])};
      o[2 * n] = pk2(h[0], h[1]); o[2 * n + 1] = pk2(h[2], h[3]);
    }
    __builtin_amdgcn_raw_buffer_store_b128(o, rH, ((pm * 16 + (i >> 2) * 8 + wr * 4 + (i & 3)) * (F / 32) + pn * 4 + wc) * 1024 + lane * 16, 0, 16);
  }
  hflag = pm * 32 + pn;
  WAIT_V(12);
#undef STAGE_A
#undef STAGE_B
#undef STAGE_S
#undef LDA
#undef LDB
#undef OPA
#undef OPB
#undef WAIT_A
#undef WAIT_B
#undef WAIT_AB
#undef MMA
#undef KTILE
  }
  STAMP(8);
  conv_run(lds, blockIdx.x * 4, dw, y2T, wid, opaque_lane(), [&] {
    WAIT_V(0);
    if (lane == 0 && __hip_atomic_fetch_add((LAS unsigned*)(lds + G1_CNT), 1u, __ATOMIC_RELAXED, __HIP_MEMORY_SCOPE_WORKGROUP) == 7u) st_agent(ctl + CT_H + hflag, tag); });
  STAMP(9);
  {
  const int lane = opaque_lane(), fr = lane & 15, fq = lane >> 4, wm = wid >> 2, wk = (wid >> 1) & 1, wn = wid & 1;
  const int voffA = ((pm2 * 8 + (wid >> 1)) * (F / 32) + kh * (F / 64) + (wid & 1)) * 1024 + lane * 16;
#define BLDS_X(rs, l, vo, so) __builtin_amdgcn_raw_ptr_buffer_load_lds((rs), (LAS void*)(l), 16, (vo), (so), 0, 16)
#define HAUX 16
#define BLDS_H(rs, l, vo, so) __builtin_amdgcn_raw_ptr_buffer_load_lds((rs), (LAS void*)(l), 16, (vo), (so), 0, HAUX)
#define STG_A(j) do { BLDS_H(rH, ldw + (((j) + 3) % 5) * G2_ST, voffA, (j) * 2048); BLDS_H(rH, ldw + (((j) + 3) % 5) * G2_ST + 8192, voffA, (j) * 2048 + 4 * (F / 32) * 1024); } while (0)
  int aoff[3], boff[3][4];
  { const int qq = fr >> 2, p = lane & 3, f = (qq & 1) | ((qq >> 1) << 2) | ((fq & 1) << 3);
#pragma unroll
    for (int i = 0; i < 3; ++i) { aoff[i] = lb + i * 2 * G2_ST + wm * 8192 + wk * 1024 + lane * 16;
      boff[i][0] = lb + i * 2 * G2_ST + 16384 + wk * 8192 + (8 * fq + 4 * (fq & 1) + qq) * 256 + (((8 * wn + p) ^ f) << 4) + ((p & 1) << 3);
      boff[i][1] = boff[i][0] ^ 8; boff[i][2] = boff[i][0] ^ 64; boff[i][3] = boff[i][0] ^ 72; } }
  const int hsw = (fq & 1) ? -1024 : 1024;
  struct Fr { bf16x8 a[4]; TrFrag b[4]; } F0, F1;
  auto ldf = [&](Fr& Z, auto J_) { constexpr int SL = (J_ + 3) % 5;
    static_for<4>([&](auto N_) { trrd<(SL & 1) * G2_ST>(Z.b[N_].lo, boff[SL >> 1][N_]); trrd<(SL & 1) * G2_ST>(Z.b[N_].hi, boff[SL >> 1][N_] + hsw); });
    static_for<4>([&](auto M_) { rd128<(SL & 1) * G2_ST + M_ * 2048>(Z.a[M_], aoff[SL >> 1]); }); };
#define WAIT_F(Z) asm volatile("s_waitcnt lgkmcnt(0)" : "+v"(Z.a[0]), "+v"(Z.a[1]), "+v"(Z.a[2]), "+v"(Z.a[3]), "+v"(Z.b[0].lo), "+v"(Z.b[0].hi), "+v"(Z.b[1].lo), "+v"(Z.b[1].hi), \
    "+v"(Z.b[2].lo), "+v"(Z.b[2].hi), "+v"(Z.b[3].lo), "+v"(Z.b[3].hi) :: "memory")
  f32x4 acc[4][4];
#pragma unroll
  for (int m = 0; m < 4; ++m)
#pragma unroll
    for (int n = 0; n < 4; ++n) acc[m][n] = (f32x4){0.f, 0.f, 0.f, 0.f};
  auto mma = [&](Fr& Z) {
#pragma unroll
    for (int m = 0; m < 4; ++m)
#pragma unroll
      for (int n = 0; n < 4; ++n) acc[m][n] = MFMA16(FRAG(Z.b[n]), Z.a[m], acc[m][n]); };
  WAIT_L(0); BAR;
  if (wid == 0) poll_flags<1>(ctl + CT_H + (pm2 >> 1) * 32 + kh * 14 + (lane < 14 ? lane : 0), tag);
  STG_B(2); STG_B(3); STG_B(4);
  BAR;
  STAMP(10);
  STG_A(0); STG_A(1); STG_A(2); STG_A(3); STG_A(4);
  WAIT_V(8); BAR;
  if (tid == 0) st_agent(ctl + CT_Y + blockIdx.x, tag);
  ldf(F0, IC<0>{});
  u64 yf = 0;
  const int yoff = ((pn2 * 128 + 8 * wid + (lane >> 3)) * S + pm2 * 128 + kh * 64 + (lane & 7) * 8) * 2;
  static_for<27>([&](auto T_) { constexpr int T = T_;
    Fr& C = (T & 1) ? F1 : F0; Fr& N = (T & 1) ? F0 : F1;
    constexpr int NV = T < 3 ? 6 + 2 * T : T < 20 ? 12 : T < 23 ? 13 : T == 23 ? 12 : T == 24 ? 10 : T == 25 ? 6 : 2;
    asm volatile("s_waitcnt vmcnt(%0)" :: "i"(NV) : "memory");
    WAIT_F(C); BAR;
    if constexpr (T == 19) ld_flag_async(yf, ctl + CT_Y + pn2 * 32 + (lane & 31));
    if constexpr (T == 23) { pin(yf); if (!__all(yf == tag)) poll_flags<1>(ctl + CT_Y + pn2 * 32 + (lane & 31), tag); }
    constexpr int SL = (T + 4) % 5, SD = (T + 3) % 5;
    static_for<16>([&](auto I_) { constexpr int I = I_, R = I - (I + 2) / 4;
      acc[I >> 2][I & 3] = MFMA16(FRAG(C.b[I & 3]), C.a[I >> 2], acc[I >> 2][I & 3]);
      if constexpr ((I & 3) == 1) {
        if constexpr (T < 23 && I < 8) BLDS_H(rH, ldw + SD * G2_ST + (I >> 2) * 8192, voffA, (T + 5) * 2048 + (I >> 2) * 4 * (F / 32) * 1024);
        if constexpr (T < 23 && I > 8) BLDS(rW, ldw + SD * G2_ST + (I >> 2) * 8192, voffW, (T + 5) * (64 * D * 2) + ((I >> 2) - 2) * 32 * D * 2);
        if constexpr (T == 23 && I < 8) BLDS_X(RSRC(y2T, S * D * 2), ldw + SD * G2_ST + (I >> 2) * 8192, yoff, (I >> 2) * 64 * S * 2);
      } else if constexpr (R < 8) trrd<(SL & 1) * G2_ST>((R & 1) ? N.b[R >> 1].hi : N.b[R >> 1].lo, (R & 1) ? boff[SL >> 1][R >> 1] + hsw : boff[SL >> 1][R >> 1]);
      else rd128<(SL & 1) * G2_ST + (R - 8) * 2048>(N.a[R - 8], aoff[SL >> 1]);
      SCHED; }); });
  const int row0 = pm2 * 128 + wm * 64 + wk * 32 + fr, col0 = pn2 * 128 + wn * 64 + fq * 8;
  u32x4 xv[2][2]; unsigned sg[2];
  if (wm == kh) {
#pragma unroll
    for (int i = 0; i < 2; ++i) { xv[i][0] = *(const u32x4*)(X + (size_t)(row0 + 16 * i) * D + col0); xv[i][1] = *(const u32x4*)(X + (size_t)(row0 + 16 * i) * D + col0 + 32);
      sg[i] = (unsigned)ld_agent(ctl + CT_S + row0 + 16 * i); }
  }
  WAIT_F(F1); mma(F1);
  STAMP(11);
  f32x4 c[2][4];
  { LAS f32x4* ro = (LAS f32x4*)(lds + 2 * G2_ST + wid * 8192) + lane; LAS f32x4* ri = (LAS f32x4*)(lds + 2 * G2_ST + (wid ^ 2) * 8192) + lane;
#pragma unroll
    for (int i = 0; i < 8; ++i) ro[i * 64] = wk ? acc[i >> 2][i & 3] : acc[2 + (i >> 2)][i & 3];
    WAIT_V(0); WAIT_L(0); BAR;
    asm volatile("" ::: "memory");
#pragma unroll
    for (int i = 0; i < 8; ++i) c[i >> 2][i & 3] = (wk ? acc[2 + (i >> 2)][i & 3] : acc[i >> 2][i & 3]) + ri[i * 64]; }
  const auto rX = RSRC(xbuf, 256 * 32768);
  if (wm != kh) {
#pragma unroll
    for (int i = 0; i < 8; ++i) __builtin_amdgcn_raw_buffer_store_b128(__builtin_bit_cast(u32x4, c[i >> 2][i & 3]), rX, blockIdx.x * 32768 + (wid & 3) * 8192 + i * 1024 + lane * 16, 0, 16);
    WAIT_V(0);
    if (lane == 0) st_agent(ctl + CT_X + blockIdx.x * 4 + (wid & 3), tag);
  } else {
    poll_flags<1>(ctl + CT_X + (blockIdx.x ^ 1) * 4 + (wid & 3), tag);
#pragma unroll
    for (int i = 0; i < 8; ++i) c[i >> 2][i & 3] += __builtin_bit_cast(f32x4, __builtin_amdgcn_raw_buffer_load_b128(rX, (blockIdx.x ^ 1) * 32768 + (wid & 3) * 8192 + i * 1024 + lane * 16, 0, 16));
    const bool odd = fq & 1;
    const int qq = fr >> 2, p = lane & 3;
#pragma unroll
    for (int i = 0; i < 2; ++i) {
      const float mu = lo16(sg[i]), ri = __builtin_amdgcn_rcpf(hi16(sg[i])), r2 = ri * ri;
#pragma unroll
      for (int g = 0; g < 2; ++g) {
        LAS unsigned char* yp = lds + 1 * G2_ST + (wn * 64 + g * 32 + fq * 8 + qq) * 128 + (wk * 32 + i * 16 + 4 * p) * 2;
        const u32x2 y0 = __builtin_bit_cast(u32x2, __builtin_amdgcn_ds_read_tr16_b64_v4i16((LAS s16x4*)yp));
        const u32x2 y1 = __builtin_bit_cast(u32x2, __builtin_amdgcn_ds_read_tr16_b64_v4i16((LAS s16x4*)(yp + 512)));
        const unsigned yv[4] = {y0[0], y0[1], y1[0], y1[1]};
        const f32x4 cl = odd ? c[i][2 * g + 1] : c[i][2 * g], ch = odd ? c[i][2 * g] : c[i][2 * g + 1];
        const float bb[8] = {cl[0], cl[1], cl[2], cl[3], ch[0], ch[1], ch[2], ch[3]};
        u32x4 o;
#pragma unroll
        for (int j = 0; j < 4; ++j) {
          const float x0 = lo16(xv[i][g][j]), x1 = hi16(xv[i][g][j]);
          o[j] = pk2(x0 + (x0 - mu) * ri * lo16(yv[j]) + bb[2 * j] * r2, x1 + (x1 - mu) * ri * hi16(yv[j]) + bb[2 * j + 1] * r2);
        }
        *(u32x4*)(out + (size_t)(row0 + 16 * i) * D + col0 + 32 * g) = o;
      }
    }
  }
  }
  if (blockIdx.x == 0 && wid == 0) {
    poll_flags<4>(ctl + CT_Y + lane, tag);
    if (lane == 0) st_agent(ctl, tag);
  }
#undef BLDS_X
#undef STG_A
#undef STG_B
#undef WAIT_F
}

extern "C" void kernel_launch_stages(void* const* d_in, void* d_out, void* d_ws, hipStream_t stream, int stages) {
  const u16* q = (const u16*)d_in[0]; const u16* k = (const u16*)d_in[1]; const u16* v = (const u16*)d_in[2];
  const u16* shk = (const u16*)d_in[3]; const u16* hk = (const u16*)d_in[4]; const u16* sD = (const u16*)d_in[5];
  const u16* Wg = (const u16*)d_in[7]; const u16* Wu = (const u16*)d_in[8]; const u16* Wd = (const u16*)d_in[9];
  char* ws = (char*)d_ws;
  u64* ctl = (u64*)ws; u16* kvT = (u16*)(ws + (1 << 20)); u16* y2T = (u16*)(ws + (5 << 20)); u16* H = (u16*)(ws + (9 << 20)); float* xbuf = (float*)(ws + (24 << 20));
  (void)hipFuncSetAttribute((const void*)k_l1, hipFuncAttributeMaxDynamicSharedMemorySize, LDS_BYTES);
  if (stages) k_l1<<<256, 512, LDS_BYTES, stream>>>(q, Wg, Wu, H, ctl, k, v, shk, hk, sD, Wd, kvT, y2T, xbuf, (u16*)d_out);
}

extern "C" void kernel_launch(void* const* d_in, const int* in_sizes, int n_in, void* d_out, int out_size, void* d_ws, size_t ws_size, hipStream_t stream) {
  kernel_launch_stages(d_in, d_out, d_ws, stream, 15);
}
```

```cpp
#include <hip/hip_runtime.h>
#include <hip/hip_bf16.h>

#define LAS __attribute__((address_space(3)))
#define DI __device__ __forceinline__
typedef unsigned short u16;
typedef short s16x4 __attribute__((ext_vector_type(4)));
typedef short bf16x8 __attribute__((ext_vector_type(8)));
typedef __bf16 bf2 __attribute__((ext_vector_type(2)));
typedef float f32x2 __attribute__((ext_vector_type(2)));
typedef float f32x4 __attribute__((ext_vector_type(4)));
typedef unsigned u32x4 __attribute__((ext_vector_type(4)));
typedef unsigned u32x2 __attribute__((ext_vector_type(2)));

constexpr int S = 2048, D = 1024, F = 3584;

template <int V> struct IC { static constexpr int value = V; constexpr operator int() const { return V; } };
template <int I, int N, class Fn> DI void static_for_(Fn&& f) { if constexpr (I < N) { f(IC<I>{}); static_for_<I + 1, N>(f); } }
template <int N, class Fn> DI void static_for(Fn&& f) { static_for_<0, N>(f); }
DI unsigned pk2(float lo, float hi) { bf2 p; p[0] = (__bf16)lo; p[1] = (__bf16)hi; return __builtin_bit_cast(unsigned, p); }
DI float lo16(unsigned v) { return __uint_as_float(v << 16); }
DI float hi16(unsigned v) { return __uint_as_float(v & 0xffff0000u); }
DI float rbf(float x) { return lo16(pk2(x, x)); }
DI float fma1(float a, float b, float c) { float r; asm("v_fma_f32 %0, %1, %2, %3" : "=v"(r) : "v"(a), "v"(b), "v"(c)); return r; }
DI float mul1(float a, float b) { float r; asm("v_mul_f32 %0, %1, %2" : "=v"(r) : "v"(a), "v"(b)); return r; }
DI float dot2(unsigned a, unsigned b, float c) { return __builtin_amdgcn_fdot2_f32_bf16(__builtin_bit_cast(bf2, a), __builtin_bit_cast(bf2, b), c, false); }
#define WAIT_V(n) asm volatile("s_waitcnt vmcnt(" #n ")" ::: "memory")
#define WAIT_L(n) asm volatile("s_waitcnt lgkmcnt(" #n ")" ::: "memory")
#define BAR __builtin_amdgcn_s_barrier()
#define SCHED __builtin_amdgcn_sched_barrier(0)
#define GLDS(g, l) __builtin_amdgcn_global_load_lds((const unsigned*)(g), (LAS unsigned*)(l), 16, 0, 0)
#define BLDS(rs, l, vo, so) __builtin_amdgcn_raw_ptr_buffer_load_lds((rs), (LAS void*)(l), 16, (vo), (so), 0, 0)
#define RSRC(p, bytes) __builtin_amdgcn_make_buffer_rsrc((void*)(p), 0, (bytes), 0x00020000)
#define MFMA16(a, b, c) __builtin_amdgcn_mfma_f32_16x16x32_bf16((a), (b), (c), 0, 0, 0)
template <int OFF> DI void rd128(bf16x8& dst, int base) { asm volatile("ds_read_b128 %0, %1 offset:%2" : "=v"(dst) : "v"(base), "i"(OFF) : "memory"); }
template <int OFF> DI void rd64(s16x4& dst, int base) { asm volatile("ds_read_b64 %0, %1 offset:%2" : "=v"(dst) : "v"(base), "i"(OFF) : "memory"); }
template <int OFF> DI void trrd(s16x4& dst, int base) { asm volatile("ds_read_b64_tr_b16 %0, %1 offset:%2" : "=v"(dst) : "v"(base), "i"(OFF) : "memory"); }
struct TrFrag { s16x4 lo, hi; };
template <int OFF> DI void trf(TrFrag& f, int base) { trrd<OFF>(f.lo, base); trrd<OFF + 1024>(f.hi, base); }
#define FRAG(f) __builtin_shufflevector((f).lo, (f).hi, 0, 1, 2, 3, 4, 5, 6, 7)

#define STAMP_DECL
#define ST_PARAM
#define ST_ARG
#define STAMP(i)
#define STAMP_OUT(kid, bid)

#define SIDE_DELAY 32
typedef unsigned long long u64;
constexpr int CT_K = 64, CT_Y = 128, CT_S = 384, CT_H = CT_S + 2048;
DI int opaque_lane() { int t = threadIdx.x; asm volatile("" : "+v"(t)); return t & 63; }
DI void ld_flag_async(u64& dst, const u64* p) { asm volatile("global_load_dwordx2 %0, %1, off sc1" : "=v"(dst) : "v"(p) : "memory"); }
DI void pin(u64& x) { asm volatile("" : "+v"(x)); }
DI u64 uniform64(u64 x) { return ((u64)(unsigned)__builtin_amdgcn_readfirstlane((unsigned)(x >> 32)) << 32) | (unsigned)__builtin_amdgcn_readfirstlane((unsigned)x); }
DI u64 ld_agent(const u64* p) { return __hip_atomic_load(p, __ATOMIC_RELAXED, __HIP_MEMORY_SCOPE_AGENT); }
DI void st_agent(u64* p, u64 x) { __hip_atomic_store(p, x, __ATOMIC_RELAXED, __HIP_MEMORY_SCOPE_AGENT); }
template <int N> DI void poll_flags(const u64* p, u64 tag, int last = 0) {
  for (int spin = 0; spin < (1 << 22); ++spin) {
    bool ok = true;
#pragma unroll
    for (int i = 0; i < N; ++i) ok &= ld_agent(p + 64 * i + (i == N - 1 ? last : 0)) == tag;
    if (__all(ok)) break;
    __builtin_amdgcn_s_sleep(2);
  }
}
template <int CTRL> DI float dpp_add(float x) { return x + __uint_as_float(__builtin_amdgcn_update_dpp(0u, __float_as_uint(x), CTRL, 0xf, 0xf, true)); }
DI float wave_sum(float x) {
  x = dpp_add<0xb1>(x); x = dpp_add<0x4e>(x); x = dpp_add<0x141>(x); x = dpp_add<0x140>(x);
  const unsigned u = __float_as_uint(x);
  return (__uint_as_float(__builtin_amdgcn_readlane(u, 0)) + __uint_as_float(__builtin_amdgcn_readlane(u, 16))) +
         (__uint_as_float(__builtin_amdgcn_readlane(u, 32)) + __uint_as_float(__builtin_amdgcn_readlane(u, 48)));
}
DI void side_prep(LAS unsigned char* lds, int hb, u64 tag, u64* ctl, const u16* __restrict__ X, const u16* __restrict__ k, const u16* __restrict__ v,
                  const u16* __restrict__ shk, u16* __restrict__ kvT ST_PARAM) {
  const int tid = threadIdx.x, wid = __builtin_amdgcn_readfirstlane(tid >> 6), lane = tid & 63;
  const int cc = tid & 15, rg = tid >> 4, s0 = (hb >> 3) * 512 + 16 * rg, c0 = (hb & 7) * 128 + 8 * cc;
  const int vo = (s0 * D + c0) * 2, vh = s0 ? vo - 3 * D * 2 : 1 << 30;
  const auto rk = RSRC(k, S * D * 2), rv = RSRC(v, S * D * 2), rx = RSRC(X, S * D * 2);
  u32x4 xr[8][2], kr[19], vr[16], wv[4];
#pragma unroll
  for (int i = 0; i < 8; ++i) { xr[i][0] = __builtin_amdgcn_raw_buffer_load_b128(rx, (hb * 64 + wid * 8) * D * 2 + lane * 16, i * D * 2, 0);
                                xr[i][1] = __builtin_amdgcn_raw_buffer_load_b128(rx, (hb * 64 + wid * 8) * D * 2 + 1024 + lane * 16, i * D * 2, 0); }
#pragma unroll
  for (int i = 0; i < 4; ++i) wv[i] = *(const u32x4*)(shk + (size_t)c0 * 4 + 8 * i);
#pragma unroll
  for (int i = 0; i < 3; ++i) kr[i] = __builtin_amdgcn_raw_buffer_load_b128(rk, vh, i * D * 2, 0);
#pragma unroll
  for (int i = 0; i < 16; ++i) { kr[3 + i] = __builtin_amdgcn_raw_buffer_load_b128(rk, vo, i * D * 2, 0); vr[i] = __builtin_amdgcn_raw_buffer_load_b128(rv, vo, i * D * 2, 0); }
  SCHED;
  STAMP(1);
#pragma unroll
  for (int r = 0; r < 8; ++r) {
    f32x2 x[8];
#pragma unroll
    for (int i = 0; i < 8; ++i) x[i] = (f32x2){lo16(xr[r][i >> 2][i & 3]), hi16(xr[r][i >> 2][i & 3])};
    const f32x2 s = ((x[0] + x[1]) + (x[2] + x[3])) + ((x[4] + x[5]) + (x[6] + x[7]));
    const float mean = wave_sum(s[0] + s[1]) * (1.0f / D);
    f32x2 q = {0.f, 0.f};
#pragma unroll
    for (int i = 0; i < 8; ++i) { const f32x2 d = x[i] - mean; q += d * d; }
    const float sd = __builtin_amdgcn_sqrtf(rbf(rbf(wave_sum(q[0] + q[1]) * (1.0f / D)) + 1e-5f));
    if (lane == 0) st_agent(ctl + CT_S + hb * 64 + wid * 8 + r, (tag << 32) | pk2(mean, sd));
    SCHED;
  }
  STAMP(2);
#pragma unroll
  for (int j = 0; j < 4; ++j) {
    const f32x2 t0 = {lo16(wv[j][0]), lo16(wv[j][2])}, t1 = {hi16(wv[j][0]), hi16(wv[j][2])}, t2 = {lo16(wv[j][1]), lo16(wv[j][3])}, t3 = {hi16(wv[j][1]), hi16(wv[j][3])};
    f32x2 kf[19], a[16];
#pragma unroll
    for (int i = 0; i < 19; ++i) kf[i] = (f32x2){lo16(kr[i][j]), hi16(kr[i][j])};
#pragma unroll
    for (int i = 0; i < 16; ++i) a[i] = (kf[i] * t3 + kf[i + 1] * t2 + kf[i + 2] * t1 + kf[i + 3] * t0) * (f32x2){lo16(vr[i][j]), hi16(vr[i][j])};
#pragma unroll
    for (int e = 0; e < 2; ++e)
#pragma unroll
      for (int h = 0; h < 2; ++h) {
        const int t = 15 - 8 * h;
        *(LAS u32x4*)(lds + (8 * cc + 2 * j + e) * 1024 + (((62 - 2 * rg + h) ^ (cc & 7)) << 4)) =
            (u32x4){pk2(a[t][e], a[t - 1][e]), pk2(a[t - 2][e], a[t - 3][e]), pk2(a[t - 4][e], a[t - 5][e]), pk2(a[t - 6][e], a[t - 7][e])};
      }
    SCHED;
  }
  STAMP(3);
  WAIT_L(0); BAR;
  asm volatile("" ::: "memory");
  const auto rt = RSRC(kvT, S * D * 2);
#pragma unroll
  for (int i = 0; i < 16; ++i) {
    const int ch = wid * 16 + i;
    const u32x4 o = *(const LAS u32x4*)(lds + ch * 1024 + ((lane ^ ((ch >> 3) & 7)) << 4));
    __builtin_amdgcn_raw_buffer_store_b128(o, rt, (((hb & 7) * 128 + ch) * S + (S - 512 - (hb >> 3) * 512) + lane * 8) * 2, 0, 16);
  }
  WAIT_V(0); BAR;
  if (tid == 0) st_agent(ctl + CT_K + (hb & 7) * 4 + (hb >> 3), tag);
  STAMP(4);
}

constexpr int CV_CP = 4160, CV_TB = 4 * CV_CP, CV_UB = 4608, CV_T0 = 4 * CV_UB, CV_RAW = CV_T0 + 3 * CV_TB, CV_RW = 4224, CV_END = CV_RAW + 4 * CV_RW + CV_TB;
template <int PART> DI void conv_fetch(LAS unsigned char* lds, int c0, const u16* __restrict__ hk, const u16* kvT, int wid, int lane) {
  if (wid < 4) {
    const auto rh = RSRC(hk + (size_t)(c0 + wid) * S, S * 2);
#pragma unroll
    for (int i = PART * 3; i < 3 + PART * 2; ++i) if (i < 4 || lane < 4) BLDS(rh, lds + CV_RAW + wid * CV_RW + i * 1024, (i * 64 + lane - 2) * 16, 0);
  } else {
#pragma unroll
    for (int i = PART * 2; i < 2 + PART * 2; ++i)
      __builtin_amdgcn_raw_ptr_buffer_load_lds(RSRC(kvT, S * D * 2), (LAS void*)(lds + (wid & 3) * CV_UB + i * 1024), 16, lane * 16, (c0 + (wid & 3)) * S * 2 + i * 1024, 0, 16);
  }
}
struct CvSet { TrFrag a[8]; bf16x8 b[8]; };
template <int M0, int J> DI void cv_read(CvSet& Z, int pa, int pb) {
  if constexpr (J < 8) rd128<(7 - J) * 512 + M0 * 64>(Z.b[J], pb);
  else { constexpr int H = J - 8; rd64<(H >> 1) * 512 + M0 * 64 + (H & 1) * 8>((H & 1) ? Z.a[H >> 1].hi : Z.a[H >> 1].lo, pa); }
}
template <int M0> DI void cv_step(f32x4 (&acc)[8], CvSet& C, CvSet& N, int pa, int pb, const bf16x8& dg) {
  asm volatile("s_waitcnt lgkmcnt(0)" : "+v"(C.a[0].lo), "+v"(C.a[0].hi), "+v"(C.a[1].lo), "+v"(C.a[1].hi), "+v"(C.a[2].lo), "+v"(C.a[2].hi), "+v"(C.a[3].lo), "+v"(C.a[3].hi),
               "+v"(C.a[4].lo), "+v"(C.a[4].hi), "+v"(C.a[5].lo), "+v"(C.a[5].hi), "+v"(C.a[6].lo), "+v"(C.a[6].hi), "+v"(C.a[7].lo), "+v"(C.a[7].hi),
               "+v"(C.b[0]), "+v"(C.b[1]), "+v"(C.b[2]), "+v"(C.b[3]), "+v"(C.b[4]), "+v"(C.b[5]), "+v"(C.b[6]), "+v"(C.b[7]) :: "memory");
  if constexpr (M0 == 0) static_for<8>([&](auto T_) { acc[T_] = MFMA16(dg, C.b[T_], ((f32x4){0.f, 0.f, 0.f, 0.f})); });
  static_for<8>([&](auto M1_) { constexpr int M1 = M1_;
    static_for<8 - M1>([&](auto T_) { constexpr int T = T_, I = M1 * 8 - M1 * (M1 - 1) / 2 + T;
      acc[M1 + T] = MFMA16(FRAG(C.a[M1]), C.b[T], acc[M1 + T]);
      if constexpr (M0 < 3 && I < 24) cv_read<M0 + 1, I>(N, pa, pb);
      SCHED; }); });
}
template <class Fill> DI void conv_run(LAS unsigned char* lds, int c0, unsigned dw, u16* y2T, int wid, int lane, Fill&& fill) {
  const int fr = lane & 15, fq = lane >> 4, d = c0 + (wid & 3), hf = wid >> 2;
  LAS unsigned char* tb = lds + CV_T0 + (wid & 3) * CV_TB + ((wid & 3) == 3 ? 4 * CV_RW : 0);
  LAS unsigned char* raw = lds + CV_RAW + (wid & 3) * CV_RW;
  LAS unsigned char* ub = lds + (wid & 3) * CV_UB;
#define CYC(i)
  if (wid < 4) {
    u32x4 rl[5]; u32x2 rh[5];
#pragma unroll
    for (int it = 0; it < 5; ++it) { rl[it] = *(const LAS u32x4*)(raw + (64 * it + (it < 4 ? lane : lane & 1)) * 16); rh[it] = *(const LAS u32x2*)(raw + (64 * it + (it < 4 ? lane : lane & 1)) * 16 + 16); }
    if (lane < 32) *(LAS u32x4*)(ub + 4096 + lane * 16) = (u32x4){0u, 0u, 0u, 0u};
#pragma unroll
    for (int it = 0; it < 5; ++it) {
      const unsigned dd[6] = {rl[it][0], rl[it][1], rl[it][2], rl[it][3], rh[it][0], rh[it][1]};
      unsigned sh[5];
#pragma unroll
      for (int i = 0; i < 5; ++i) sh[i] = __builtin_amdgcn_alignbit(dd[i + 1], dd[i], 16);
      LAS unsigned char* p = tb + (64 * it + (it < 4 ? lane : lane & 1)) * 16;
      *(LAS u32x4*)(p) = (u32x4){sh[0], sh[1], sh[2], sh[3]};
      *(LAS u32x4*)(p + CV_CP) = (u32x4){dd[1], dd[2], dd[3], dd[4]};
      *(LAS u32x4*)(p + 2 * CV_CP) = (u32x4){sh[1], sh[2], sh[3], sh[4]};
      *(LAS u32x4*)(p + 3 * CV_CP) = (u32x4){dd[2], dd[3], dd[4], dd[5]};
    }
  }
  const int pa = (int)(unsigned)(size_t)tb + (fr & 3) * CV_CP + ((fr >> 2) * 4 + 8 * fq) * 2 + hf * 256;
  const int pb = (int)(unsigned)(size_t)ub + (2032 - 16 * fr + 8 * fq) * 2 - 7 * 512 + hf * 256;
  const int dj = hf ? -2 : 15 - fr - 8 * fq;
  const unsigned db = ((d & 1) ? dw >> 16 : dw & 0xffffu) << ((dj & 1) * 16);
  const u32x4 dgw = {(dj >> 1) == 0 ? db : 0u, (dj >> 1) == 1 ? db : 0u, (dj >> 1) == 2 ? db : 0u, (dj >> 1) == 3 ? db : 0u};
  const bf16x8 dg = __builtin_bit_cast(bf16x8, dgw);
  if (wid < 4) fill();
  WAIT_L(0); CYC(0); BAR;
  CYC(1);
  f32x4 acc[8];
  { CvSet X, Y;
    static_for<24>([&](auto J_) { cv_read<0, J_>(X, pa, pb); });
    cv_step<0>(acc, X, Y, pa, pb, dg); if (wid >= 4) fill(); cv_step<1>(acc, Y, X, pa, pb, dg); cv_step<2>(acc, X, Y, pa, pb, dg); cv_step<3>(acc, Y, X, pa, pb, dg); }
  auto slot = [&](int w) { return (LAS f32x4*)(lds + (w < 4 ? CV_RAW : w < 6 ? CV_END - 4 * 4096 : 112 * 1024) + w * 4096) + lane; };
  LAS f32x4* xo = slot(wid); LAS f32x4* xi = slot(wid ^ 4);
#pragma unroll
  for (int i = 0; i < 4; ++i) xo[i * 64] = hf ? acc[i] : acc[4 + i];
  WAIT_L(0); CYC(2); BAR;
  CYC(3);
  asm volatile("" ::: "memory");
#pragma unroll
  for (int i = 0; i < 4; ++i) {
    const f32x4 a = (hf ? acc[4 + i] : acc[i]) + xi[i * 64];
    __builtin_amdgcn_raw_buffer_store_b64((u32x2){pk2(a[0], a[1]), pk2(a[2], a[3])}, RSRC(y2T, S * D * 2), d * S * 2 + (16 * fr + 4 * fq) * 2 + hf * 2048 + i * 512, 0, 16);
  }
  CYC(4);
}

constexpr int HTB = 16384, G1_CS = 8 * HTB, G1_SG = G1_CS + 2048, G1_CNT = G1_SG + 3072, LDS_BYTES = 160 * 1024;
constexpr int G2_ST = 32768;
__global__ __launch_bounds__(512, 2) void k_l1(const u16* __restrict__ X, const u16* __restrict__ Wg, const u16* __restrict__ Wu, u16* H, u64* ctl,
                                               const u16* __restrict__ k, const u16* __restrict__ v, const u16* __restrict__ shk, const u16* __restrict__ hk,
                                               const u16* __restrict__ sD, const u16* __restrict__ Wd, u16* kvT, u16* y2T, float* xbuf, u16* __restrict__ out) {
  extern __shared__ __attribute__((aligned(16))) unsigned char smem[];
  LAS unsigned char* lds = (LAS unsigned char*)smem;
  STAMP_DECL; STAMP(0);
  const int tid = threadIdx.x, wid = __builtin_amdgcn_readfirstlane(tid >> 6), lane = tid & 63, wr = wid >> 2, wc = wid & 3, fr = lane & 15, fq = lane >> 4;
  const u64 epoch = ld_agent(ctl);
  const unsigned dw = ((const __attribute__((address_space(4))) unsigned*)sD)[blockIdx.x * 2 + (wc >> 1)];
  const int lb = (int)(unsigned)(size_t)lds;
  LAS unsigned char* ldw = lds + wid * 1024;
  const auto rH = RSRC(H, S * F * 2);
  u64 tag;
  int hflag;
  const int kh = blockIdx.x & 1, pm2 = ((blockIdx.x >> 1) & 3) * 4 + ((blockIdx.x >> 3) & 3), pn2 = blockIdx.x >> 5;
  int voffW;
  auto set_voffW = [&] { const int ln = opaque_lane(), row = 4 * wid + (ln >> 4), f = (row & 1) | (((row >> 1) & 1) << 2) | (((row >> 3) & 1) << 3), ch = (ln & 15) ^ f;
    voffW = (kh * (F / 2) + row) * D * 2 + pn2 * 256 + ch * 16; };
  const auto rW = RSRC(Wd, F * D * 2);
#define STG_B(j) do { BLDS(rW, ldw + (((j) + 3) % 5) * G2_ST + 16384, voffW, (j) * (64 * D * 2)); BLDS(rW, ldw + (((j) + 3) % 5) * G2_ST + 24576, voffW, (j) * (64 * D * 2) + 32 * D * 2); } while (0)
  if (tid == 0) *(LAS unsigned*)(lds + G1_CNT) = 0u;
  if (blockIdx.x < 32) {
    tag = uniform64(epoch) + 1;
    __builtin_amdgcn_s_sleep(SIDE_DELAY);
    side_prep(lds, blockIdx.x, tag, ctl, X, k, v, shk, kvT ST_ARG);
    poll_flags<1>(ctl + CT_K + (lane & 3), tag);
    conv_fetch<0>(lds, blockIdx.x * 4, hk, kvT, wid, lane); conv_fetch<1>(lds, blockIdx.x * 4, hk, kvT, wid, lane);
#pragma unroll
    for (int i = 0; i < 4; ++i) { int ln = (blockIdx.x * 8 + wid) * 224 + i * 64 + lane; ln = (i == 3 && lane >= 32) ? ln - 64 : ln;
      __builtin_amdgcn_global_load_lds((const unsigned*)Wd + (size_t)ln * 32, (LAS unsigned*)(lds + G1_CNT + 256), 4, 0, 0); }
    WAIT_V(0);
    set_voffW(); STG_B(0); STG_B(1);
    hflag = (blockIdx.x & 7) * 32 + 28 + (blockIdx.x >> 3);
  } else {
  const int xg = blockIdx.x & 7, idx = (blockIdx.x >> 3) - 4, pm = (xg & 1) * 4 + (idx & 3), pn = (xg >> 1) * 7 + (idx >> 2);
  const int krot = (xg & 1) * 8;
  int voffA, voffB;
  { const int row = 8 * wid + (lane >> 3), ch = (lane & 7) ^ ((row >> 1) & 7); voffA = (pm * 256 + row) * D * 2 + ch * 16; }
  { const int row = 4 * wid + (lane >> 4), f = (row & 1) | (((row >> 1) & 1) << 2) | (((row >> 3) & 1) << 3), ch = (lane & 15) ^ f; voffB = row * F * 2 + pn * 256 + ch * 16; }
  const auto rA = RSRC(X, S * D * 2), rG = RSRC(Wg, D * F * 2), rU = RSRC(Wu, D * F * 2);
  const int aoff0 = lb + (wr * 64 + fr) * 128 + (((fq + 4 * wr) ^ (fr >> 1)) << 4), aoff1 = aoff0 ^ 64;
  int boff[2][2];
  { const int qq = fr >> 2, p = lane & 3, f = (qq & 1) | ((qq >> 1) << 2) | ((fq & 1) << 3);
    boff[0][0] = lb + 4 * HTB + wr * 8192 + (8 * fq + qq) * 256 + (((4 * wc + p) ^ f) << 4) + ((p & 1) << 3);
    boff[1][0] = boff[0][0] ^ 8; boff[0][1] = boff[0][0] ^ 8192; boff[1][1] = boff[1][0] ^ 8192; }
#define STAGE_A(b, h, kt) do { BLDS(rA, ldw + ((b) * 2 + (h)) * HTB, voffA, (h) * (128 * D * 2) + ((kt) ^ krot) * 128); \
    BLDS(rA, ldw + ((b) * 2 + (h)) * HTB + 8192, voffA, (h) * (128 * D * 2) + 64 * D * 2 + ((kt) ^ krot) * 128); } while (0)
#define STAGE_B(b, h, kt) do { BLDS((h) ? rU : rG, ldw + (4 + (b) * 2 + (h)) * HTB, voffB, ((kt) ^ krot) * (64 * F * 2)); \
    BLDS((h) ? rU : rG, ldw + (4 + (b) * 2 + (h)) * HTB + 8192, voffB, ((kt) ^ krot) * (64 * F * 2) + 32 * F * 2); } while (0)
#define LDA(b, h) do { static_for<4>([&](auto M_) { rd128<((b) * 2 + (h)) * HTB + M_ * 2048>(At[M_][0], aoff0); rd128<((b) * 2 + (h)) * HTB + M_ * 2048>(At[M_][1], aoff1); }); } while (0)
#define LDB(dst, b, h) do { trf<((b) * 2 + (h)) * HTB>(dst[0][0], boff[0][0]); trf<((b) * 2 + (h)) * HTB>(dst[1][0], boff[1][0]); \
    trf<((b) * 2 + (h)) * HTB>(dst[0][1], boff[0][1]); trf<((b) * 2 + (h)) * HTB>(dst[1][1], boff[1][1]); } while (0)
#define OPA "+v"(At[0][0]), "+v"(At[0][1]), "+v"(At[1][0]), "+v"(At[1][1]), "+v"(At[2][0]), "+v"(At[2][1]), "+v"(At[3][0]), "+v"(At[3][1])
#define OPB(Bt) "+v"(Bt[0][0].lo), "+v"(Bt[0][0].hi), "+v"(Bt[0][1].lo), "+v"(Bt[0][1].hi), "+v"(Bt[1][0].lo), "+v"(Bt[1][0].hi), "+v"(Bt[1][1].lo), "+v"(Bt[1][1].hi)
#define WAIT_A() asm volatile("s_waitcnt lgkmcnt(0)" : OPA :: "memory")
#define WAIT_B(Bt) asm volatile("s_waitcnt lgkmcnt(0)" : OPB(Bt) :: "memory")
#define WAIT_AB(Bt) asm volatile("s_waitcnt lgkmcnt(0)" : OPA, OPB(Bt) :: "memory")
#define MMA(ai, bj, Bt) do { __builtin_amdgcn_s_setprio(1); _Pragma("unroll") for (int kk = 0; kk < 2; ++kk) _Pragma("unroll") for (int m = 0; m < 4; ++m) _Pragma("unroll") for (int n = 0; n < 2; ++n) \
    acc[ai][bj][m][n] = MFMA16(FRAG(Bt[n][kk]), At[m][kk], acc[ai][bj][m][n]); \
    if (ai) { cs[bj][0] = MFMA16(FRAG(Bt[0][0]), ones, cs[bj][0]); cs[bj][1] = MFMA16(FRAG(Bt[1][0]), ones, cs[bj][1]); } __builtin_amdgcn_s_setprio(0); } while (0)
  STAGE_B(0, 0, 0); STAGE_A(0, 0, 0); STAGE_B(0, 1, 0); STAGE_A(0, 1, 0); STAGE_B(1, 0, 1); STAGE_A(1, 0, 1); STAGE_B(1, 1, 1);
  f32x4 acc[2][2][4][2], cs[2][2];
#pragma unroll
  for (int a = 0; a < 2; ++a)
#pragma unroll
    for (int b = 0; b < 2; ++b) {
      cs[a][b] = (f32x4){0.f, 0.f, 0.f, 0.f};
#pragma unroll
      for (int m = 0; m < 4; ++m)
#pragma unroll
        for (int n = 0; n < 2; ++n) acc[a][b][m][n] = (f32x4){0.f, 0.f, 0.f, 0.f};
    }
  bf16x8 ones = {0x3f80, 0x3f80, 0x3f80, 0x3f80, 0x3f80, 0x3f80, 0x3f80, 0x3f80};
  asm volatile("" : "+v"(ones));
  bf16x8 At[4][2]; TrFrag B0[2][2], B1[2][2];
  constexpr int NT = D / 64;
  WAIT_V(6);
  tag = uniform64(epoch) + 1;
  BAR;
  if (wr == 1) BAR;
  STAMP(6);
#define KTILE(b, P1, P2, P3, P4, W4) do { \
    LDB(B0, b, 0); LDA(b, 0); P1; SCHED; WAIT_L(8); BAR; WAIT_AB(B0); MMA(0, 0, B0); BAR; SCHED; \
    LDB(B1, b, 1); P2; SCHED; BAR; WAIT_B(B1); MMA(0, 1, B1); BAR; SCHED; \
    LDA(b, 1); P3; SCHED; BAR; WAIT_A(); MMA(1, 0, B0); BAR; SCHED; \
    P4; SCHED; W4; BAR; MMA(1, 1, B1); BAR; SCHED; } while (0)
  for (int t = 0; t < NT - 2; t += 2) {
    KTILE(0, STAGE_A(1, 1, t + 1), STAGE_B(0, 0, t + 2), STAGE_A(0, 0, t + 2), STAGE_B(0, 1, t + 2), WAIT_V(6));
    KTILE(1, STAGE_A(0, 1, t + 2), STAGE_B(1, 0, t + 3), STAGE_A(1, 0, t + 3), STAGE_B(1, 1, t + 3), WAIT_V(6));
  }
#define STAGE_S() do { if (wid < 3) __builtin_amdgcn_raw_ptr_buffer_load_lds(RSRC(ctl, CT_H * 8), (LAS void*)(ldw + G1_SG), 16, lane * 16, \
    wid < 2 ? CT_S * 8 + pm * 2048 + wid * 1024 : CT_K * 8, 0, 16); } while (0)
  KTILE(0, STAGE_A(1, 1, NT - 1), STAGE_S(), (void)0, (void)0, WAIT_V(0));
#define FETCH_U() do { const int ln = opaque_lane(); \
    if (!__all(*(const LAS u64*)(lds + G1_SG + 2048 + ((blockIdx.x >> 5) * 4 + (ln & 3)) * 8) == tag)) poll_flags<1>(ctl + CT_K + (blockIdx.x >> 5) * 4 + (ln & 3), tag); \
    conv_fetch<0>(lds, blockIdx.x * 4, hk, kvT, wid, ln); } while (0)
  KTILE(1, (void)0, FETCH_U(), conv_fetch<1>(lds, blockIdx.x * 4, hk, kvT, wid, opaque_lane()), do { set_voffW(); STG_B(0); STG_B(1); } while (0), (void)0);
#undef FETCH_U
  STAMP(7);
  LAS f32x4* cst = (LAS f32x4*)(lds + G1_CS);
  if (fr == 0) {
#pragma unroll
    for (int i = 0; i < 4; ++i) cst[(wid * 4 + i) * 4 + fq] = cs[i >> 1][i & 1];
  }
  WAIT_L(0);
  if (wr == 0) BAR;
  BAR;
  asm volatile("" ::: "memory");
#pragma unroll
  for (int i = 0; i < 4; ++i) cs[i >> 1][i & 1] += cst[((wid ^ 4) * 4 + i) * 4 + fq];
  unsigned sg[8];
  { bool ok = true;
#pragma unroll
    for (int i = 0; i < 8; ++i) { const u32x2 g = *(const LAS u32x2*)(lds + G1_SG + ((i >> 2) * 128 + wr * 64 + (i & 3) * 16 + fr) * 8); sg[i] = g[0]; ok &= g[1] == (unsigned)tag; }
    if (__builtin_expect(!__all(ok), 0)) {
      for (int spin = 0; spin < (1 << 22); ++spin) {
        ok = true;
#pragma unroll
        for (int i = 0; i < 8; ++i) { const u64 g = ld_agent(ctl + CT_S + pm * 256 + (i >> 2) * 128 + wr * 64 + (i & 3) * 16 + fr); sg[i] = (unsigned)g; ok &= (unsigned)(g >> 32) == (unsigned)tag; }
        if (__all(ok)) break;
      }
    } }
#pragma unroll
  for (int i = 0; i < 8; ++i) {
    const float mu = lo16(sg[i]), c1 = __builtin_amdgcn_rcpf(hi16(sg[i])) * -1.44269504089f;
    u32x4 o;
#pragma unroll
    for (int n = 0; n < 2; ++n) {
      const f32x4 g = acc[i >> 2][0][i & 3][n] - cs[0][n] * mu, u = acc[i >> 2][1][i & 3][n] - cs[1][n] * mu, e = g * c1;
      f32x4 t = {__builtin_amdgcn_exp2f(e[0]), __builtin_amdgcn_exp2f(e[1]), __builtin_amdgcn_exp2f(e[2]), __builtin_amdgcn_exp2f(e[3])};
      t += 1.0f;
      const f32x4 h = g * u * (f32x4){__builtin_amdgcn_rcpf(t[0]), __builtin_amdgcn_rcpf(t[1]), __builtin_amdgcn_rcpf(t[2]), __builtin_amdgcn_rcpf(t[3])};
      o[2 * n] = pk2(h[0], h[1]); o[2 * n + 1] = pk2(h[2], h[3]);
    }
    __builtin_amdgcn_raw_buffer_store_b128(o, rH, ((pm * 16 + (i >> 2) * 8 + wr * 4 + (i & 3)) * (F / 32) + pn * 4 + wc) * 1024 + lane * 16, 0, 16);
  }
  hflag = pm * 32 + pn;
  WAIT_V(12);
#undef STAGE_A
#undef STAGE_B
#undef STAGE_S
#undef LDA
#undef LDB
#undef OPA
#undef OPB
#undef WAIT_A
#undef WAIT_B
#undef WAIT_AB
#undef MMA
#undef KTILE
  }
  STAMP(8);
  conv_run(lds, blockIdx.x * 4, dw, y2T, wid, opaque_lane(), [&] {
    WAIT_V(0);
    if (lane == 0 && __hip_atomic_fetch_add((LAS unsigned*)(lds + G1_CNT), 1u, __ATOMIC_RELAXED, __HIP_MEMORY_SCOPE_WORKGROUP) == 7u) st_agent(ctl + CT_H + hflag, tag); });
  STAMP(9);
  {
  const int lane = opaque_lane(), fr = lane & 15, fq = lane >> 4, wm = wid >> 2, wk = (wid >> 1) & 1, wn = wid & 1;
  const int voffA = ((pm2 * 8 + (wid >> 1)) * (F / 32) + kh * (F / 64) + (wid & 1)) * 1024 + lane * 16;
#define BLDS_X(rs, l, vo, so) __builtin_amdgcn_raw_ptr_buffer_load_lds((rs), (LAS void*)(l), 16, (vo), (so), 0, 16)
#define HAUX 16
#define BLDS_H(rs, l, vo, so) __builtin_amdgcn_raw_ptr_buffer_load_lds((rs), (LAS void*)(l), 16, (vo), (so), 0, HAUX)
#define STG_A(j) do { BLDS_H(rH, ldw + (((j) + 3) % 5) * G2_ST, voffA, (j) * 2048); BLDS_H(rH, ldw + (((j) + 3) % 5) * G2_ST + 8192, voffA, (j) * 2048 + 4 * (F / 32) * 1024); } while (0)
  int aoff[3], boff[3][4];
  { const int qq = fr >> 2, p = lane & 3, f = (qq & 1) | ((qq >> 1) << 2) | ((fq & 1) << 3);
#pragma unroll
    for (int i = 0; i < 3; ++i) { aoff[i] = lb + i * 2 * G2_ST + wm * 8192 + wk * 1024 + lane * 16;
      boff[i][0] = lb + i * 2 * G2_ST + 16384 + wk * 8192 + (8 * fq + 4 * (fq & 1) + qq) * 256 + (((8 * wn + p) ^ f) << 4) + ((p & 1) << 3);
      boff[i][1] = boff[i][0] ^ 8; boff[i][2] = boff[i][0] ^ 64; boff[i][3] = boff[i][0] ^ 72; } }
  const int hsw = (fq & 1) ? -1024 : 1024;
  struct Fr { bf16x8 a[4]; TrFrag b[4]; } F0, F1;
  auto ldf = [&](Fr& Z, auto J_) { constexpr int SL = (J_ + 3) % 5;
    static_for<4>([&](auto N_) { trrd<(SL & 1) * G2_ST>(Z.b[N_].lo, boff[SL >> 1][N_]); trrd<(SL & 1) * G2_ST>(Z.b[N_].hi, boff[SL >> 1][N_] + hsw); });
    static_for<4>([&](auto M_) { rd128<(SL & 1) * G2_ST + M_ * 2048>(Z.a[M_], aoff[SL >> 1]); }); };
#define WAIT_F(Z) asm volatile("s_waitcnt lgkmcnt(0)" : "+v"(Z.a[0]), "+v"(Z.a[1]), "+v"(Z.a[2]), "+v"(Z.a[3]), "+v"(Z.b[0].lo), "+v"(Z.b[0].hi), "+v"(Z.b[1].lo), "+v"(Z.b[1].hi), \
    "+v"(Z.b[2].lo), "+v"(Z.b[2].hi), "+v"(Z.b[3].lo), "+v"(Z.b[3].hi) :: "memory")
  f32x4 acc[4][4];
#pragma unroll
  for (int m = 0; m < 4; ++m)
#pragma unroll
    for (int n = 0; n < 4; ++n) acc[m][n] = (f32x4){0.f, 0.f, 0.f, 0.f};
  auto mma = [&](Fr& Z) {
#pragma unroll
    for (int m = 0; m < 4; ++m)
#pragma unroll
      for (int n = 0; n < 4; ++n) acc[m][n] = MFMA16(FRAG(Z.b[n]), Z.a[m], acc[m][n]); };
  WAIT_L(0); BAR;
  if (wid == 0) poll_flags<1>(ctl + CT_H + (pm2 >> 1) * 32 + kh * 14 + (lane < 14 ? lane : 0), tag);
  STG_B(2); STG_B(3); STG_B(4);
  BAR;
  STAMP(10);
  STG_A(0); STG_A(1); STG_A(2); STG_A(3); STG_A(4);
  WAIT_V(8); BAR;
  if (tid == 0) st_agent(ctl + CT_Y + blockIdx.x, tag);
  ldf(F0, IC<0>{});
  u64 yf = 0;
  const int yoff = ((pn2 * 128 + 8 * wid + (lane >> 3)) * S + pm2 * 128 + kh * 64 + (lane & 7) * 8) * 2;
  static_for<27>([&](auto T_) { constexpr int T = T_;
    Fr& C = (T & 1) ? F1 : F0; Fr& N = (T & 1) ? F0 : F1;
    constexpr int NV = T < 3 ? 6 + 2 * T : T < 20 ? 12 : T < 23 ? 13 : T == 23 ? 12 : T == 24 ? 10 : T == 25 ? 6 : 2;
    asm volatile("s_waitcnt vmcnt(%0)" :: "i"(NV) : "memory");
    WAIT_F(C); BAR;
    if constexpr (T == 19) ld_flag_async(yf, ctl + CT_Y + pn2 * 32 + (lane & 31));
    if constexpr (T == 23) { pin(yf); if (!__all(yf == tag)) poll_flags<1>(ctl + CT_Y + pn2 * 32 + (lane & 31), tag); }
    constexpr int SL = (T + 4) % 5, SD = (T + 3) % 5;
    static_for<16>([&](auto I_) { constexpr int I = I_, R = I - (I + 2) / 4;
      acc[I >> 2][I & 3] = MFMA16(FRAG(C.b[I & 3]), C.a[I >> 2], acc[I >> 2][I & 3]);
      if constexpr ((I & 3) == 1) {
        if constexpr (T < 23 && I < 8) BLDS_H(rH, ldw + SD * G2_ST + (I >> 2) * 8192, voffA, (T + 5) * 2048 + (I >> 2) * 4 * (F / 32) * 1024);
        if constexpr (T < 23 && I > 8) BLDS(rW, ldw + SD * G2_ST + (I >> 2) * 8192, voffW, (T + 5) * (64 * D * 2) + ((I >> 2) - 2) * 32 * D * 2);
        if constexpr (T == 23 && I < 8) BLDS_X(RSRC(y2T, S * D * 2), ldw + SD * G2_ST + (I >> 2) * 8192, yoff, (I >> 2) * 64 * S * 2);
      } else if constexpr (R < 8) trrd<(SL & 1) * G2_ST>((R & 1) ? N.b[R >> 1].hi : N.b[R >> 1].lo, (R & 1) ? boff[SL >> 1][R >> 1] + hsw : boff[SL >> 1][R >> 1]);
      else rd128<(SL & 1) * G2_ST + (R - 8) * 2048>(N.a[R - 8], aoff[SL >> 1]);
      SCHED; }); });
  const int row0 = pm2 * 128 + wm * 64 + wk * 32 + fr, col0 = pn2 * 128 + wn * 64 + fq * 8;
  u32x4 xv[2][2]; unsigned sg[2];
  if (wm == kh) {
#pragma unroll
    for (int i = 0; i < 2; ++i) { xv[i][0] = *(const u32x4*)(X + (size_t)(row0 + 16 * i) * D + col0); xv[i][1] = *(const u32x4*)(X + (size_t)(row0 + 16 * i) * D + col0 + 32);
      sg[i] = (unsigned)ld_agent(ctl + CT_S + row0 + 16 * i); }
  }
  WAIT_F(F1); mma(F1);
  STAMP(11);
  f32x4 c[2][4];
  { LAS f32x4* ro = (LAS f32x4*)(lds + 2 * G2_ST + wid * 8192) + lane; LAS f32x4* ri = (LAS f32x4*)(lds + 2 * G2_ST + (wid ^ 2) * 8192) + lane;
#pragma unroll
    for (int i = 0; i < 8; ++i) ro[i * 64] = wk ? acc[i >> 2][i & 3] : acc[2 + (i >> 2)][i & 3];
    WAIT_V(0); WAIT_L(0); BAR;
    asm volatile("" ::: "memory");
#pragma unroll
    for (int i = 0; i < 8; ++i) c[i >> 2][i & 3] = (wk ? acc[2 + (i >> 2)][i & 3] : acc[i >> 2][i & 3]) + ri[i * 64]; }
  const auto rX = RSRC(xbuf, 256 * 32768);
  if (blockIdx.x >= 32) STAMP(2);
  if (wm != kh) {
#pragma unroll
    for (int i = 0; i < 8; ++i) { const f32x4 t = c[i >> 2][i & 3];
      __builtin_amdgcn_raw_buffer_store_b128((u32x4){pk2(t[0], t[1]), (unsigned)tag, pk2(t[2], t[3]), (unsigned)tag}, rX, blockIdx.x * 32768 + (wid & 3) * 8192 + i * 1024 + lane * 16, 0, 16); }
  } else {
    u32x4 g[8];
    const int go = (blockIdx.x ^ 1) * 32768 + (wid & 3) * 8192 + lane * 16;
    for (int spin = 0; spin < (1 << 22); ++spin) {
      g[7] = __builtin_amdgcn_raw_buffer_load_b128(rX, go + 7 * 1024, 0, 16);
      if (__all(g[7][1] == (unsigned)tag && g[7][3] == (unsigned)tag)) break;
      asm volatile("s_sleep 2" ::: "memory");
    }
    if (blockIdx.x >= 32) STAMP(3);
    for (int spin = 0; spin < (1 << 20); ++spin) {
      bool ok = true;
#pragma unroll
      for (int i = 0; i < 7; ++i) g[i] = __builtin_amdgcn_raw_buffer_load_b128(rX, go + i * 1024, 0, 16);
#pragma unroll
      for (int i = 0; i < 7; ++i) ok &= g[i][1] == (unsigned)tag && g[i][3] == (unsigned)tag;
      if (__all(ok)) break;
      asm volatile("" ::: "memory");
    }
    if (blockIdx.x >= 32) STAMP(4);
#pragma unroll
    for (int i = 0; i < 8; ++i) c[i >> 2][i & 3] += (f32x4){lo16(g[i][0]), hi16(g[i][0]), lo16(g[i][2]), hi16(g[i][2])};
    const bool odd = fq & 1;
    const int qq = fr >> 2, p = lane & 3;
#pragma unroll
    for (int i = 0; i < 2; ++i) {
      const float mu = lo16(sg[i]), ri = __builtin_amdgcn_rcpf(hi16(sg[i])), r2 = ri * ri;
#pragma unroll
      for (int g = 0; g < 2; ++g) {
        LAS unsigned char* yp = lds + 1 * G2_ST + (wn * 64 + g * 32 + fq * 8 + qq) * 128 + (wk * 32 + i * 16 + 4 * p) * 2;
        const u32x2 y0 = __builtin_bit_cast(u32x2, __builtin_amdgcn_ds_read_tr16_b64_v4i16((LAS s16x4*)yp));
        const u32x2 y1 = __builtin_bit_cast(u32x2, __builtin_amdgcn_ds_read_tr16_b64_v4i16((LAS s16x4*)(yp + 512)));
        const unsigned yv[4] = {y0[0], y0[1], y1[0], y1[1]};
        const f32x4 cl = odd ? c[i][2 * g + 1] : c[i][2 * g], ch = odd ? c[i][2 * g] : c[i][2 * g + 1];
        const float bb[8] = {cl[0], cl[1], cl[2], cl[3], ch[0], ch[1], ch[2], ch[3]};
        u32x4 o;
#pragma unroll
        for (int j = 0; j < 4; ++j) {
          const float x0 = lo16(xv[i][g][j]), x1 = hi16(xv[i][g][j]);
          o[j] = pk2(x0 + (x0 - mu) * ri * lo16(yv[j]) + bb[2 * j] * r2, x1 + (x1 - mu) * ri * hi16(yv[j]) + bb[2 * j + 1] * r2);
        }
        *(u32x4*)(out + (size_t)(row0 + 16 * i) * D + col0 + 32 * g) = o;
      }
    }
  }
  }
  if (blockIdx.x == 0 && wid == 0) {
    poll_flags<4>(ctl + CT_Y + lane, tag);
    if (lane == 0) st_agent(ctl, tag);
  }
#undef BLDS_X
#undef STG_A
#undef STG_B
#undef WAIT_F
}

extern "C" void kernel_launch_stages(void* const* d_in, void* d_out, void* d_ws, hipStream_t stream, int stages) {
  const u16* q = (const u16*)d_in[0]; const u16* k = (const u16*)d_in[1]; const u16* v = (const u16*)d_in[2];
  const u16* shk = (const u16*)d_in[3]; const u16* hk = (const u16*)d_in[4]; const u16* sD = (const u16*)d_in[5];
  const u16* Wg = (const u16*)d_in[7]; const u16* Wu = (const u16*)d_in[8]; const u16* Wd = (const u16*)d_in[9];
  char* ws = (char*)d_ws;
  u64* ctl = (u64*)ws; u16* kvT = (u16*)(ws + (1 << 20)); u16* y2T = (u16*)(ws + (5 << 20)); u16* H = (u16*)(ws + (9 << 20)); float* xbuf = (float*)(ws + (24 << 20));
  (void)hipFuncSetAttribute((const void*)k_l1, hipFuncAttributeMaxDynamicSharedMemorySize, LDS_BYTES);
  if (stages) k_l1<<<256, 512, LDS_BYTES, stream>>>(q, Wg, Wu, H, ctl, k, v, shk, hk, sD, Wd, kvT, y2T, xbuf, (u16*)d_out);
}

extern "C" void kernel_launch(void* const* d_in, const int* in_sizes, int n_in, void* d_out, int out_size, void* d_ws, size_t ws_size, hipStream_t stream) {
  kernel_launch_stages(d_in, d_out, d_ws, stream, 15);
}
```

```cpp
#include <hip/hip_runtime.h>
#include <hip/hip_bf16.h>

#define LAS __attribute__((address_space(3)))
#define DI __device__ __forceinline__
typedef unsigned short u16;
typedef short s16x4 __attribute__((ext_vector_type(4)));
typedef short bf16x8 __attribute__((ext_vector_type(8)));
typedef __bf16 bf2 __attribute__((ext_vector_type(2)));
typedef float f32x2 __attribute__((ext_vector_type(2)));
typedef float f32x4 __attribute__((ext_vector_type(4)));
typedef unsigned u32x4 __attribute__((ext_vector_type(4)));
typedef unsigned u32x2 __attribute__((ext_vector_type(2)));

constexpr int S = 2048, D = 1024, F = 3584;

template <int V> struct IC { static constexpr int value = V; constexpr operator int() const { return V; } };
template <int I, int N, class Fn> DI void static_for_(Fn&& f) { if constexpr (I < N) { f(IC<I>{}); static_for_<I + 1, N>(f); } }
template <int N, class Fn> DI void static_for(Fn&& f) { static_for_<0, N>(f); }
DI unsigned pk2(float lo, float hi) { bf2 p; p[0] = (__bf16)lo; p[1] = (__bf16)hi; return __builtin_bit_cast(unsigned, p); }
DI float lo16(unsigned v) { return __uint_as_float(v << 16); }
DI float hi16(unsigned v) { return __uint_as_float(v & 0xffff0000u); }
DI float rbf(float x) { return lo16(pk2(x, x)); }
#define WAIT_V(n) asm volatile("s_waitcnt vmcnt(" #n ")" ::: "memory")
#define WAIT_L(n) asm volatile("s_waitcnt lgkmcnt(" #n ")" ::: "memory")
#define BAR __builtin_amdgcn_s_barrier()
#define SCHED __builtin_amdgcn_sched_barrier(0)
#define BLDS(rs, l, vo, so) __builtin_amdgcn_raw_ptr_buffer_load_lds((rs), (LAS void*)(l), 16, (vo), (so), 0, 0)
#define RSRC(p, bytes) __builtin_amdgcn_make_buffer_rsrc((void*)(p), 0, (bytes), 0x00020000)
#define MFMA16(a, b, c) __builtin_amdgcn_mfma_f32_16x16x32_bf16((a), (b), (c), 0, 0, 0)
template <int OFF> DI void rd128(bf16x8& dst, int base) { asm volatile("ds_read_b128 %0, %1 offset:%2" : "=v"(dst) : "v"(base), "i"(OFF) : "memory"); }
template <int OFF> DI void rd64(s16x4& dst, int base) { asm volatile("ds_read_b64 %0, %1 offset:%2" : "=v"(dst) : "v"(base), "i"(OFF) : "memory"); }
template <int OFF> DI void trrd(s16x4& dst, int base) { asm volatile("ds_read_b64_tr_b16 %0, %1 offset:%2" : "=v"(dst) : "v"(base), "i"(OFF) : "memory"); }
struct TrFrag { s16x4 lo, hi; };
template <int OFF> DI void trf(TrFrag& f, int base) { trrd<OFF>(f.lo, base); trrd<OFF + 1024>(f.hi, base); }
#define FRAG(f) __builtin_shufflevector((f).lo, (f).hi, 0, 1, 2, 3, 4, 5, 6, 7)


typedef unsigned long long u64;
constexpr int CT_K = 64, CT_Y = 128, CT_S = 384, CT_H = CT_S + 2048, CT_C = 4096;
DI int opaque_lane() { int t = threadIdx.x; asm volatile("" : "+v"(t)); return t & 63; }
DI u64 ld_agent(const u64* p) { return __hip_atomic_load(p, __ATOMIC_RELAXED, __HIP_MEMORY_SCOPE_AGENT); }
DI void st_agent(u64* p, u64 x) { __hip_atomic_store(p, x, __ATOMIC_RELAXED, __HIP_MEMORY_SCOPE_AGENT); }
template <int N> DI void poll_flags(const u64* p, u64 tag) {
  for (int spin = 0; spin < (1 << 22); ++spin) {
    bool ok = true;
#pragma unroll
    for (int i = 0; i < N; ++i) ok &= ld_agent(p + 64 * i) == tag;
    if (__all(ok)) break;
    __builtin_amdgcn_s_sleep(2);
  }
}
template <int CTRL> DI float dpp_add(float x) { return x + __uint_as_float(__builtin_amdgcn_update_dpp(0u, __float_as_uint(x), CTRL, 0xf, 0xf, true)); }
DI float wave_sum(float x) {
  x = dpp_add<0xb1>(x); x = dpp_add<0x4e>(x); x = dpp_add<0x141>(x); x = dpp_add<0x140>(x);
  const unsigned u = __float_as_uint(x);
  return (__uint_as_float(__builtin_amdgcn_readlane(u, 0)) + __uint_as_float(__builtin_amdgcn_readlane(u, 16))) +
         (__uint_as_float(__builtin_amdgcn_readlane(u, 32)) + __uint_as_float(__builtin_amdgcn_readlane(u, 48)));
}
DI void side_prep(LAS unsigned char* lds, int hb, u64 tag, u64* ctl, const u16* __restrict__ X, const u16* __restrict__ k, const u16* __restrict__ v,
                  const u16* __restrict__ shk, u16* __restrict__ kvT) {
  const int tid = threadIdx.x, wid = __builtin_amdgcn_readfirstlane(tid >> 6), lane = tid & 63;
  const int cc = tid & 15, rg = tid >> 4, s0 = (hb >> 3) * 512 + 16 * rg, c0 = (hb & 7) * 128 + 8 * cc;
  const int vo = (s0 * D + c0) * 2, vh = s0 ? vo - 3 * D * 2 : 1 << 30;
  const auto rk = RSRC(k, S * D * 2), rv = RSRC(v, S * D * 2), rx = RSRC(X, S * D * 2);
  u32x4 xr[8][2], kr[19], vr[16], wv[4];
#pragma unroll
  for (int i = 0; i < 4; ++i) wv[i] = *(const u32x4*)(shk + (size_t)c0 * 4 + 8 * i);
#pragma unroll
  for (int i = 0; i < 3; ++i) kr[i] = __builtin_amdgcn_raw_buffer_load_b128(rk, vh, i * D * 2, 0);
#pragma unroll
  for (int i = 0; i < 16; ++i) { kr[3 + i] = __builtin_amdgcn_raw_buffer_load_b128(rk, vo, i * D * 2, 0); vr[i] = __builtin_amdgcn_raw_buffer_load_b128(rv, vo, i * D * 2, 0); }
  SCHED;
#pragma unroll
  for (int j = 0; j < 4; ++j) {
    const f32x2 t0 = {lo16(wv[j][0]), lo16(wv[j][2])}, t1 = {hi16(wv[j][0]), hi16(wv[j][2])}, t2 = {lo16(wv[j][1]), lo16(wv[j][3])}, t3 = {hi16(wv[j][1]), hi16(wv[j][3])};
    f32x2 kf[19], a[16];
#pragma unroll
    for (int i = 0; i < 19; ++i) kf[i] = (f32x2){lo16(kr[i][j]), hi16(kr[i][j])};
#pragma unroll
    for (int i = 0; i < 16; ++i) a[i] = (kf[i] * t3 + kf[i + 1] * t2 + kf[i + 2] * t1 + kf[i + 3] * t0) * (f32x2){lo16(vr[i][j]), hi16(vr[i][j])};
#pragma unroll
    for (int e = 0; e < 2; ++e)
#pragma unroll
      for (int h = 0; h < 2; ++h) {
        const int t = 15 - 8 * h;
        *(LAS u32x4*)(lds + (8 * cc + 2 * j + e) * 1024 + (((62 - 2 * rg + h) ^ (cc & 7)) << 4)) =
            (u32x4){pk2(a[t][e], a[t - 1][e]), pk2(a[t - 2][e], a[t - 3][e]), pk2(a[t - 4][e], a[t - 5][e]), pk2(a[t - 6][e], a[t - 7][e])};
      }
    SCHED;
    if (j == 0) {
#pragma unroll
      for (int i = 0; i < 8; ++i) { xr[i][0] = __builtin_amdgcn_raw_buffer_load_b128(rx, (hb * 64 + wid * 8) * D * 2 + lane * 16, i * D * 2, 0);
                                    xr[i][1] = __builtin_amdgcn_raw_buffer_load_b128(rx, (hb * 64 + wid * 8) * D * 2 + 1024 + lane * 16, i * D * 2, 0); }
      SCHED;
    }
  }
  WAIT_L(0); BAR;
  asm volatile("" ::: "memory");
  const auto rt = RSRC(kvT, S * D * 2);
#pragma unroll
  for (int r = 0; r < 8; ++r) {
#pragma unroll
    for (int i = 2 * r; i < 2 * r + 2; ++i) {
      const int ch = wid * 16 + i;
      const u32x4 o = *(const LAS u32x4*)(lds + ch * 1024 + ((lane ^ ((ch >> 3) & 7)) << 4));
      __builtin_amdgcn_raw_buffer_store_b128(o, rt, (((hb & 7) * 128 + ch) * S + (S - 512 - (hb >> 3) * 512) + lane * 8) * 2, 0, 16);
    }
    f32x2 x[8];
#pragma unroll
    for (int i = 0; i < 8; ++i) x[i] = (f32x2){lo16(xr[r][i >> 2][i & 3]), hi16(xr[r][i >> 2][i & 3])};
    const f32x2 s = ((x[0] + x[1]) + (x[2] + x[3])) + ((x[4] + x[5]) + (x[6] + x[7]));
    const float mean = wave_sum(s[0] + s[1]) * (1.0f / D);
    f32x2 q = {0.f, 0.f};
#pragma unroll
    for (int i = 0; i < 8; ++i) { const f32x2 d = x[i] - mean; q += d * d; }
    const float sd = __builtin_amdgcn_sqrtf(rbf(rbf(wave_sum(q[0] + q[1]) * (1.0f / D)) + 1e-5f));
    if (lane == 0) st_agent(ctl + CT_S + hb * 64 + wid * 8 + r, (tag << 32) | pk2(mean, sd));
    SCHED;
  }
  WAIT_V(0); BAR;
  if (tid == 0) st_agent(ctl + CT_K + (hb & 7) * 4 + (hb >> 3), tag);
}

constexpr int CV_CP = 4160, CV_TB = 4 * CV_CP, CV_UB = 4608, CV_T0 = 4 * CV_UB, CV_RAW = CV_T0 + 4 * CV_TB, CV_RW = 4224, CV_CNT = CV_UB - 16;
template <int PART> DI void conv_fetch(LAS unsigned char* lds, int c0, const u16* __restrict__ hk, const u16* kvT, int wid, int lane) {
  if (wid < 4) {
    const auto rh = RSRC(hk + (size_t)(c0 + wid) * S, S * 2);
#pragma unroll
    for (int i = PART * 3; i < 3 + PART * 2; ++i) if (i < 4 || lane < 4) __builtin_amdgcn_raw_ptr_buffer_load_lds(rh, (LAS void*)(lds + CV_RAW + wid * CV_RW + i * 1024), 16, (i * 64 + lane - 2) * 16, 0, 0, 0);
  } else {
#pragma unroll
    for (int i = PART * 2; i < 2 + PART * 2; ++i)
      __builtin_amdgcn_raw_ptr_buffer_load_lds(RSRC(kvT, S * D * 2), (LAS void*)(lds + (wid & 3) * CV_UB + i * 1024), 16, lane * 16, (c0 + (wid & 3)) * S * 2 + i * 1024, 0, 16);
  }
}
struct CvSet { TrFrag a[8]; bf16x8 b[8]; };
template <int M0, int J> DI void cv_read(CvSet& Z, int pa, int pb) {
  if constexpr (J < 8) rd128<(7 - J) * 512 + M0 * 64>(Z.b[J], pb);
  else { constexpr int H = J - 8; rd64<(H >> 1) * 512 + M0 * 64 + (H & 1) * 8>((H & 1) ? Z.a[H >> 1].hi : Z.a[H >> 1].lo, pa); }
}
template <int M0> DI void cv_step(f32x4 (&acc)[8], CvSet& C, CvSet& N, int pa, int pb, const bf16x8& dg) {
  asm volatile("s_waitcnt lgkmcnt(0)" : "+v"(C.a[0].lo), "+v"(C.a[0].hi), "+v"(C.a[1].lo), "+v"(C.a[1].hi), "+v"(C.a[2].lo), "+v"(C.a[2].hi), "+v"(C.a[3].lo), "+v"(C.a[3].hi),
               "+v"(C.a[4].lo), "+v"(C.a[4].hi), "+v"(C.a[5].lo), "+v"(C.a[5].hi), "+v"(C.a[6].lo), "+v"(C.a[6].hi), "+v"(C.a[7].lo), "+v"(C.a[7].hi),
               "+v"(C.b[0]), "+v"(C.b[1]), "+v"(C.b[2]), "+v"(C.b[3]), "+v"(C.b[4]), "+v"(C.b[5]), "+v"(C.b[6]), "+v"(C.b[7]) :: "memory");
  if constexpr (M0 == 0) static_for<8>([&](auto T_) { acc[T_] = MFMA16(dg, C.b[T_], ((f32x4){0.f, 0.f, 0.f, 0.f})); });
  static_for<8>([&](auto M1_) { constexpr int M1 = M1_;
    static_for<8 - M1>([&](auto T_) { constexpr int T = T_, I = M1 * 8 - M1 * (M1 - 1) / 2 + T;
      acc[M1 + T] = MFMA16(FRAG(C.a[M1]), C.b[T], acc[M1 + T]);
      if constexpr (M0 < 3 && I < 24) cv_read<M0 + 1, I>(N, pa, pb);
      SCHED; }); });
}
template <class Fill, class Idle, class Done> DI void conv_run(LAS unsigned char* lds, int c0, unsigned dw, u16* y2T, int wid, int lane, Fill&& fill, Idle&& idle, Done&& done) {
  const int fr = lane & 15, fq = lane >> 4, d = c0 + (wid & 3), hf = wid >> 2;
  LAS unsigned char* tb = lds + CV_T0 + (wid & 3) * CV_TB;
  LAS unsigned char* raw = lds + CV_RAW + (wid & 3) * CV_RW;
  LAS unsigned char* ub = lds + (wid & 3) * CV_UB;
  if (wid < 4) {
    u32x4 rl[5]; u32x2 rh[5];
#pragma unroll
    for (int it = 0; it < 5; ++it) { rl[it] = *(const LAS u32x4*)(raw + (64 * it + (it < 4 ? lane : lane & 1)) * 16); rh[it] = *(const LAS u32x2*)(raw + (64 * it + (it < 4 ? lane : lane & 1)) * 16 + 16); }
    if (lane < 32) *(LAS u32x4*)(ub + 4096 + lane * 16) = (u32x4){0u, 0u, 0u, 0u};
#pragma unroll
    for (int it = 0; it < 5; ++it) {
      const unsigned dd[6] = {rl[it][0], rl[it][1], rl[it][2], rl[it][3], rh[it][0], rh[it][1]};
      unsigned sh[5];
#pragma unroll
      for (int i = 0; i < 5; ++i) sh[i] = __builtin_amdgcn_alignbit(dd[i + 1], dd[i], 16);
      LAS unsigned char* p = tb + (64 * it + (it < 4 ? lane : lane & 1)) * 16;
      *(LAS u32x4*)(p) = (u32x4){sh[0], sh[1], sh[2], sh[3]};
      *(LAS u32x4*)(p + CV_CP) = (u32x4){dd[1], dd[2], dd[3], dd[4]};
      *(LAS u32x4*)(p + 2 * CV_CP) = (u32x4){sh[1], sh[2], sh[3], sh[4]};
      *(LAS u32x4*)(p + 3 * CV_CP) = (u32x4){dd[2], dd[3], dd[4], dd[5]};
    }
  }
  const int pa = (int)(unsigned)(size_t)tb + (fr & 3) * CV_CP + ((fr >> 2) * 4 + 8 * fq) * 2 + hf * 256;
  const int pb = (int)(unsigned)(size_t)ub + (2032 - 16 * fr + 8 * fq) * 2 - 7 * 512 + hf * 256;
  const int dj = hf ? -2 : 15 - fr - 8 * fq;
  const unsigned db = ((d & 1) ? dw >> 16 : dw & 0xffffu) << ((dj & 1) * 16);
  const u32x4 dgw = {(dj >> 1) == 0 ? db : 0u, (dj >> 1) == 1 ? db : 0u, (dj >> 1) == 2 ? db : 0u, (dj >> 1) == 3 ? db : 0u};
  const bf16x8 dg = __builtin_bit_cast(bf16x8, dgw);
  WAIT_L(0); BAR;
  fill();
  f32x4 acc[8];
  { CvSet X, Y;
    static_for<24>([&](auto J_) { cv_read<0, J_>(X, pa, pb); });
    cv_step<0>(acc, X, Y, pa, pb, dg); cv_step<1>(acc, Y, X, pa, pb, dg); cv_step<2>(acc, X, Y, pa, pb, dg); cv_step<3>(acc, Y, X, pa, pb, dg); }
  if (wid < 4) idle();
  BAR;
  LAS f32x4* xo = (LAS f32x4*)(lds + 65536 + wid * 4096) + lane; LAS f32x4* xi = (LAS f32x4*)(lds + 65536 + (wid ^ 4) * 4096) + lane;
#pragma unroll
  for (int i = 0; i < 4; ++i) xo[i * 64] = hf ? acc[i] : acc[4 + i];
  done();
  WAIT_L(0); BAR;
  asm volatile("" ::: "memory");
#pragma unroll
  for (int i = 0; i < 4; ++i) {
    const f32x4 a = (hf ? acc[4 + i] : acc[i]) + xi[i * 64];
    __builtin_amdgcn_raw_buffer_store_b64((u32x2){pk2(a[0], a[1]), pk2(a[2], a[3])}, RSRC(y2T, S * D * 2), d * S * 2 + (16 * fr + 4 * fq) * 2 + hf * 2048 + i * 512, 0, 16);
  }
}

constexpr int HTB = 16384, G1_CS = 8 * HTB, G1_SG = G1_CS + 2048, G1_CP = 136 * 1024, LDS_BYTES = 160 * 1024;
constexpr int G2_ST = 32768;
__global__ __launch_bounds__(512, 2) void k_fused(const u16* __restrict__ X, const u16* __restrict__ Wg, const u16* __restrict__ Wu, u16* H, u64* ctl,
                                               const u16* __restrict__ k, const u16* __restrict__ v, const u16* __restrict__ shk, const u16* __restrict__ hk,
                                               const u16* __restrict__ sD, const u16* __restrict__ Wd, u16* kvT, u16* y2T, float* xbuf, u16* __restrict__ out) {
  extern __shared__ __attribute__((aligned(16))) unsigned char smem[];
  LAS unsigned char* lds = (LAS unsigned char*)smem;
  const int tid = threadIdx.x, wid = __builtin_amdgcn_readfirstlane(tid >> 6), lane = tid & 63, wr = wid >> 2, wc = wid & 3, fr = lane & 15, fq = lane >> 4;
  u64 tag; unsigned dw;
  auto scalars = [&] { const u64* c = ctl; const u16* d = sD; asm volatile("" : "+s"(c), "+s"(d));
    tag = *(const __attribute__((address_space(4))) u64*)c * 6364136223846793005ull + 1442695040888963407ull; dw = ((const __attribute__((address_space(4))) unsigned*)d)[blockIdx.x * 2 + (wc >> 1)]; };
  const int lb = (int)(unsigned)(size_t)lds;
  LAS unsigned char* ldw = lds + wid * 1024;
  const auto rH = RSRC(H, S * F * 2);
  int hflag;
  const int kh = blockIdx.x & 1, pm2 = ((blockIdx.x >> 1) & 3) * 4 + ((blockIdx.x >> 3) & 3), pn2 = blockIdx.x >> 5;
  int voffW;
  auto set_voffW = [&] { const int ln = opaque_lane(), row = 4 * wid + (ln >> 4), f = (row & 1) | (((row >> 1) & 1) << 2) | (((row >> 3) & 1) << 3), ch = (ln & 15) ^ f;
    voffW = (kh * (F / 2) + row) * D * 2 + pn2 * 256 + ch * 16; };
  const auto rW = RSRC(Wd, F * D * 2);
#define STG_B(j) do { BLDS(rW, ldw + (((j) + 3) % 5) * G2_ST + 16384, voffW, (j) * (64 * D * 2)); BLDS(rW, ldw + (((j) + 3) % 5) * G2_ST + 24576, voffW, (j) * (64 * D * 2) + 32 * D * 2); } while (0)
  if (blockIdx.x < 32) {
    scalars();
    asm volatile("" : "+s"(tag), "+s"(dw));
    __builtin_amdgcn_s_sleep(32);
    side_prep(lds, blockIdx.x, tag, ctl, X, k, v, shk, kvT);
    poll_flags<1>(ctl + CT_K + (lane & 3), tag);
    conv_fetch<0>(lds, blockIdx.x * 4, hk, kvT, wid, lane); conv_fetch<1>(lds, blockIdx.x * 4, hk, kvT, wid, lane);
#pragma unroll
    for (int i = 0; i < 4; ++i) { int ln = (blockIdx.x * 8 + wid) * 224 + i * 64 + lane; ln = (i == 3 && lane >= 32) ? ln - 64 : ln;
      __builtin_amdgcn_global_load_lds((const unsigned*)Wd + (size_t)ln * 32, (LAS unsigned*)(lds + G1_CS), 4, 0, 0); }
    WAIT_V(0);
    set_voffW(); STG_B(0); STG_B(1);
    hflag = (blockIdx.x & 7) * 32 + 28 + (blockIdx.x >> 3);
  } else {
  const int xg = blockIdx.x & 7, idx = (blockIdx.x >> 3) - 4, pm = (xg & 1) * 4 + (idx & 3), pn = (xg >> 1) * 7 + (idx >> 2);
  const int krot = (xg & 1) * 9 + (xg >> 1) * 2;
  int voffA, voffB;
  { const int row = 8 * wid + (lane >> 3), ch = (lane & 7) ^ ((row >> 1) & 7); voffA = (pm * 256 + row) * D * 2 + ch * 16; }
  { const int row = 4 * wid + (lane >> 4), f = (row & 1) | (((row >> 1) & 1) << 2) | (((row >> 3) & 1) << 3), ch = (lane & 15) ^ f; voffB = row * F * 2 + pn * 256 + ch * 16; }
  const auto rA = RSRC(X, S * D * 2), rG = RSRC(Wg, D * F * 2), rU = RSRC(Wu, D * F * 2);
  int aoff0 = lb + (wr * 64 + fr) * 128 + ((fq ^ (fr >> 1)) << 4), aoff1 = aoff0 ^ 64;
  int boff0, boff1;
  { const int qq = fr >> 2, p = lane & 3, f = (qq & 1) | ((qq >> 1) << 2) | ((fq & 1) << 3);
    boff0 = lb + 4 * HTB + (8 * fq + qq) * 256 + (((4 * wc + p) ^ f) << 4) + ((p & 1) << 3); boff1 = boff0 ^ 8; }
#define STAGE_A(b, h, kt) do { BLDS(rA, ldw + ((b) * 2 + (h)) * HTB, voffA, (h) * (128 * D * 2) + ((kt) ^ krot) * 128); \
    BLDS(rA, ldw + ((b) * 2 + (h)) * HTB + 8192, voffA, (h) * (128 * D * 2) + 64 * D * 2 + ((kt) ^ krot) * 128); } while (0)
#define STAGE_B(b, h, kt) do { BLDS((h) ? rU : rG, ldw + (4 + (b) * 2 + (h)) * HTB, voffB, ((kt) ^ krot) * (64 * F * 2)); \
    BLDS((h) ? rU : rG, ldw + (4 + (b) * 2 + (h)) * HTB + 8192, voffB, ((kt) ^ krot) * (64 * F * 2) + 32 * F * 2); } while (0)
#define LDA(b, h) do { static_for<4>([&](auto M_) { rd128<((b) * 2 + (h)) * HTB + M_ * 2048>(At[M_][0], aoff0); rd128<((b) * 2 + (h)) * HTB + M_ * 2048>(At[M_][1], aoff1); }); } while (0)
#define LDB(dst, b, h) do { trf<((b) * 2 + (h)) * HTB>(dst[0][0], boff0); trf<((b) * 2 + (h)) * HTB>(dst[1][0], boff1); \
    trf<((b) * 2 + (h)) * HTB + 8192>(dst[0][1], boff0); trf<((b) * 2 + (h)) * HTB + 8192>(dst[1][1], boff1); } while (0)
#define OPA "+v"(At[0][0]), "+v"(At[0][1]), "+v"(At[1][0]), "+v"(At[1][1]), "+v"(At[2][0]), "+v"(At[2][1]), "+v"(At[3][0]), "+v"(At[3][1])
#define OPB(Bt) "+v"(Bt[0][0].lo), "+v"(Bt[0][0].hi), "+v"(Bt[0][1].lo), "+v"(Bt[0][1].hi), "+v"(Bt[1][0].lo), "+v"(Bt[1][0].hi), "+v"(Bt[1][1].lo), "+v"(Bt[1][1].hi)
#define WAIT_A() asm volatile("s_waitcnt lgkmcnt(0)" : OPA :: "memory")
#define WAIT_B(Bt) asm volatile("s_waitcnt lgkmcnt(0)" : OPB(Bt) :: "memory")
#define WAIT_AB(Bt) asm volatile("s_waitcnt lgkmcnt(0)" : OPA, OPB(Bt) :: "memory")
#define MMA(ai, bj, Bt) do { SCHED; static_for<16>([&](auto I_) { constexpr int m = I_ >> 2, n = (I_ >> 1) & 1, kk = I_ & 1; \
    acc[ai][bj][m][n] = MFMA16(FRAG(Bt[n][kk]), At[m][kk], acc[ai][bj][m][n]); SCHED; }); } while (0)
#define CSUM() do { if (docs) { _Pragma("unroll") for (int i = 0; i < 4; ++i) { cs[0][i & 1] = MFMA16(FRAG(B0[i & 1][i >> 1]), ones, cs[0][i & 1]); cs[1][i & 1] = MFMA16(FRAG(B1[i & 1][i >> 1]), ones, cs[1][i & 1]); } } } while (0)
  STAGE_B(0, 0, 0); STAGE_A(0, 0, 0); STAGE_B(0, 1, 0); STAGE_A(0, 1, 0); STAGE_B(1, 0, 1); STAGE_A(1, 0, 1); STAGE_B(1, 1, 1);
  scalars();
  f32x4 acc[2][2][4][2], cs[2][2];
#pragma unroll
  for (int a = 0; a < 2; ++a)
#pragma unroll
    for (int b = 0; b < 2; ++b) {
      cs[a][b] = (f32x4){0.f, 0.f, 0.f, 0.f};
#pragma unroll
      for (int m = 0; m < 4; ++m)
#pragma unroll
        for (int n = 0; n < 2; ++n) { acc[a][b][m][n] = (f32x4){0.f, 0.f, 0.f, 0.f}; asm volatile("" : "+v"(acc[a][b][m][n])); }
      asm volatile("" : "+v"(cs[a][b]));
    }
  bf16x8 ones = {0x3f80, 0x3f80, 0x3f80, 0x3f80, 0x3f80, 0x3f80, 0x3f80, 0x3f80};
  asm volatile("" : "+v"(ones));
  bf16x8 At[4][2]; TrFrag B0[2][2], B1[2][2];
  constexpr int NT = D / 64;
  asm volatile("" : "+v"(aoff0), "+v"(aoff1), "+v"(boff0), "+v"(boff1));
  WAIT_V(10);
  asm volatile("" : "+s"(tag), "+s"(dw));
  BAR;
  if (wr == 1) { __builtin_amdgcn_s_setprio(1); BAR; }
#define KTILE(b, P1, P2, P3, P4, W3, W4, NX) do { \
    LDA(b, 0); P1; SCHED; WAIT_V(10); BAR; WAIT_AB(B0); MMA(0, 0, B0); BAR; SCHED; \
    LDB(B1, b, 1); P2; SCHED; WAIT_V(10); BAR; WAIT_B(B1); MMA(0, 1, B1); BAR; SCHED; \
    LDA(b, 1); P3; SCHED; W3; BAR; WAIT_A(); MMA(1, 0, B0); BAR; SCHED; \
    P4; SCHED; if (NX) LDB(B0, (b) ^ 1, 0); SCHED; W4; BAR; if (NX) WAIT_B(B0); MMA(1, 1, B1); BAR; SCHED; } while (0)
  LDB(B0, 0, 0);
  for (int t = 0; t < NT - 2; t += 2) {
    const bool docs = t == 2 * (pm & 3) && wr == 0;
    KTILE(0, STAGE_A(1, 1, t + 1), STAGE_B(0, 0, t + 2), STAGE_A(0, 0, t + 2), do { STAGE_B(0, 1, t + 2); CSUM(); } while (0), WAIT_V(10), WAIT_V(6), 1);
    KTILE(1, STAGE_A(0, 1, t + 2), STAGE_B(1, 0, t + 3), STAGE_A(1, 0, t + 3), do { STAGE_B(1, 1, t + 3); CSUM(); } while (0), WAIT_V(10), WAIT_V(6), 1);
    if (docs) {
      const int ln = opaque_lane();
      const f32x4 q0 = (ln & 4) ? cs[0][1] : cs[0][0], q1 = (ln & 4) ? cs[1][1] : cs[1][0], q = (ln & 8) ? q1 : q0;
      const float v0 = (ln & 1) ? q[1] : q[0], v1 = (ln & 1) ? q[3] : q[2];
      st_agent(ctl + CT_C + (pn * 8 + pm) * 256 + (wc * 4 + ((ln & 15) >> 2)) * 16 + (ln >> 4) * 4 + (ln & 3), (tag << 32) | __float_as_uint((ln & 2) ? v1 : v0));
    }
  }
#define STAGE_S() do { if (wid < 3) __builtin_amdgcn_raw_ptr_buffer_load_lds(RSRC(ctl, CT_H * 8), (LAS void*)(ldw + G1_SG), 16, lane * 16, \
    wid < 2 ? CT_S * 8 + pm * 2048 + wid * 1024 : CT_K * 8, 0, 16); } while (0)
#define STAGE_C() do { const int ln = opaque_lane(); _Pragma("unroll") for (int i = 0; i < 2; ++i) \
    __builtin_amdgcn_raw_ptr_buffer_load_lds(RSRC(ctl + CT_C + pn * 2048, 16384), (LAS void*)(ldw + G1_CP + i * 8192), 16, ln * 16, wid * 1024 + i * 8192, 0, 16); } while (0)
  KTILE(0, STAGE_A(1, 1, NT - 1), do { STAGE_S(); STAGE_C(); } while (0), (void)0, (void)0, WAIT_V(8), WAIT_V(0), 1);
#define SUM_C() do { const int t_ = opaque_lane() + wid * 64; u32x2 c_[4]; bool ok = true; \
    _Pragma("unroll") for (int i = 0; i < 4; ++i) { c_[i] = *(const LAS u32x2*)(lds + G1_CP + ((t_ >> 8) * 4 + i) * 2048 + (t_ & 255) * 8); ok &= c_[i][1] == (unsigned)tag; } \
    if (__builtin_expect(!__all(ok), 0)) for (int spin = 0; spin < (1 << 22); ++spin) { ok = true; \
      _Pragma("unroll") for (int i = 0; i < 4; ++i) { const u64 g = ld_agent(ctl + CT_C + pn * 2048 + ((t_ >> 8) * 4 + i) * 256 + (t_ & 255)); c_[i][0] = (unsigned)g; ok &= (unsigned)(g >> 32) == (unsigned)tag; } \
      if (__all(ok)) break; } \
    ((LAS float*)(lds + G1_CS))[t_] = (__uint_as_float(c_[0][0]) + __uint_as_float(c_[1][0])) + (__uint_as_float(c_[2][0]) + __uint_as_float(c_[3][0])); } while (0)
#define FETCH_U() do { const int ln = opaque_lane(); \
    if (!__all(*(const LAS u64*)(lds + G1_SG + 2048 + ((blockIdx.x >> 5) * 4 + (ln & 3)) * 8) == tag)) poll_flags<1>(ctl + CT_K + (blockIdx.x >> 5) * 4 + (ln & 3), tag); \
    conv_fetch<0>(lds, blockIdx.x * 4, hk, kvT, wid, ln); } while (0)
  KTILE(1, SUM_C(), FETCH_U(), conv_fetch<1>(lds, blockIdx.x * 4, hk, kvT, wid, opaque_lane()), do { set_voffW(); STG_B(0); STG_B(1); } while (0), (void)0, (void)0, 0);
#undef FETCH_U
#undef STAGE_C
#undef SUM_C
  __builtin_amdgcn_s_setprio(0);
  const LAS f32x4* cst = (const LAS f32x4*)(lds + G1_CS);
  WAIT_L(0);
  if (wr == 0) BAR;
  BAR;
  asm volatile("" ::: "memory");
#pragma unroll
  for (int i = 0; i < 4; ++i) cs[i >> 1][i & 1] = cst[(wc * 4 + i) * 4 + fq] + cst[64 + (wc * 4 + i) * 4 + fq];
  unsigned sg[8];
  { bool ok = true;
#pragma unroll
    for (int i = 0; i < 8; ++i) { const u32x2 g = *(const LAS u32x2*)(lds + G1_SG + ((i >> 2) * 128 + wr * 64 + (i & 3) * 16 + fr) * 8); sg[i] = g[0]; ok &= g[1] == (unsigned)tag; }
    if (__builtin_expect(!__all(ok), 0)) {
      for (int spin = 0; spin < (1 << 22); ++spin) {
        ok = true;
#pragma unroll
        for (int i = 0; i < 8; ++i) { const u64 g = ld_agent(ctl + CT_S + pm * 256 + (i >> 2) * 128 + wr * 64 + (i & 3) * 16 + fr); sg[i] = (unsigned)g; ok &= (unsigned)(g >> 32) == (unsigned)tag; }
        if (__all(ok)) break;
      }
    } }
#pragma unroll
  for (int i = 0; i < 8; ++i) {
    const float mu = lo16(sg[i]), c1 = __builtin_amdgcn_rcpf(hi16(sg[i])) * -1.44269504089f;
    u32x4 o;
#pragma unroll
    for (int n = 0; n < 2; ++n) {
      const f32x4 g = acc[i >> 2][0][i & 3][n] - cs[0][n] * mu, u = acc[i >> 2][1][i & 3][n] - cs[1][n] * mu, e = g * c1;
      f32x4 t = {__builtin_amdgcn_exp2f(e[0]), __builtin_amdgcn_exp2f(e[1]), __builtin_amdgcn_exp2f(e[2]), __builtin_amdgcn_exp2f(e[3])};
      t += 1.0f;
      const f32x4 h = g * u * (f32x4){__builtin_amdgcn_rcpf(t[0]), __builtin_amdgcn_rcpf(t[1]), __builtin_amdgcn_rcpf(t[2]), __builtin_amdgcn_rcpf(t[3])};
      o[2 * n] = pk2(h[0], h[1]); o[2 * n + 1] = pk2(h[2], h[3]);
    }
    __builtin_amdgcn_raw_buffer_store_b128(o, rH, ((pm * 16 + (i >> 2) * 8 + wr * 4 + (i & 3)) * (F / 32) + pn * 4 + wc) * 1024 + lane * 16, 0, 16);
  }
  hflag = pm * 32 + pn;
  WAIT_V(12);
#undef STAGE_A
#undef STAGE_B
#undef STAGE_S
#undef LDA
#undef LDB
#undef OPA
#undef OPB
#undef WAIT_A
#undef WAIT_B
#undef WAIT_AB
#undef MMA
#undef CSUM
#undef KTILE
  }
  const int voffA = ((pm2 * 8 + (wid >> 1)) * (F / 32) + kh * (F / 64) + (wid & 1)) * 1024 + opaque_lane() * 16;
#define BLDS_X(rs, l, vo, so) __builtin_amdgcn_raw_ptr_buffer_load_lds((rs), (LAS void*)(l), 16, (vo), (so), 0, 16)
#define STG_A(j) do { BLDS_X(rH, ldw + (((j) + 3) % 5) * G2_ST, voffA, (j) * 2048); BLDS_X(rH, ldw + (((j) + 3) % 5) * G2_ST + 8192, voffA, (j) * 2048 + 4 * (F / 32) * 1024); } while (0)
  conv_run(lds, blockIdx.x * 4, dw, y2T, wid, opaque_lane(), [&] {
    WAIT_V(0);
    if (lane == 0 && __hip_atomic_fetch_add((LAS unsigned*)(lds + CV_CNT), 1u, __ATOMIC_RELAXED, __HIP_MEMORY_SCOPE_WORKGROUP) == 7u) st_agent(ctl + CT_H + hflag, tag); },
    [&] {
      const int ln = opaque_lane();
      if (blockIdx.x < 32) { for (int spin = 0; spin < (1 << 20) && ld_agent(ctl + CT_H + (pm2 >> 1) * 32 + kh * 14) != tag; ++spin) __builtin_amdgcn_s_sleep(16); }
      poll_flags<1>(ctl + CT_H + (pm2 >> 1) * 32 + kh * 14 + (ln < 14 ? ln : 0), tag);
#pragma unroll
      for (int i = 0; i < 8; ++i)
        __builtin_amdgcn_raw_ptr_buffer_load_lds(rH, (LAS void*)(ldw + (3 + (i >> 2)) * G2_ST + (i & 3) * 4096), 16, voffA, (i >> 2) * 2048 + (i & 3) * 2 * (F / 32) * 1024, 0, 16); },
    [&] { STG_A(2); STG_B(2); STG_A(3); STG_B(3); });
  {
  const int lane = opaque_lane(), fr = lane & 15, fq = lane >> 4, wm = wid >> 2, wk = (wid >> 1) & 1, wn = wid & 1;
  int aoff[3], boff[3][4];
  { const int qq = fr >> 2, p = lane & 3, f = (qq & 1) | ((qq >> 1) << 2) | ((fq & 1) << 3);
#pragma unroll
    for (int i = 0; i < 3; ++i) { aoff[i] = lb + i * 2 * G2_ST + wm * 8192 + wk * (4096 + 1024) + lane * 16;
      boff[i][0] = lb + i * 2 * G2_ST + 16384 + wk * 8192 + (8 * fq + 4 * (fq & 1) + qq) * 256 + (((8 * wn + p) ^ f) << 4) + ((p & 1) << 3);
      boff[i][1] = boff[i][0] ^ 8; boff[i][2] = boff[i][0] ^ 64; boff[i][3] = boff[i][0] ^ 72; } }
  const int hsw = (fq & 1) ? -1024 : 1024;
  struct Fr { bf16x8 a[4]; TrFrag b[4]; } F0, F1;
  auto ldf = [&](Fr& Z, auto J_) { constexpr int SL = (J_ + 3) % 5;
    static_for<4>([&](auto N_) { trrd<(SL & 1) * G2_ST>(Z.b[N_].lo, boff[SL >> 1][N_]); trrd<(SL & 1) * G2_ST>(Z.b[N_].hi, boff[SL >> 1][N_] + hsw); });
    static_for<4>([&](auto M_) { rd128<(SL & 1) * G2_ST + (M_ & 1) * 2048>(Z.a[M_], aoff[SL >> 1] ^ ((M_ >> 1) * 4096)); }); };
#define WAIT_F(Z) asm volatile("s_waitcnt lgkmcnt(0)" : "+v"(Z.a[0]), "+v"(Z.a[1]), "+v"(Z.a[2]), "+v"(Z.a[3]), "+v"(Z.b[0].lo), "+v"(Z.b[0].hi), "+v"(Z.b[1].lo), "+v"(Z.b[1].hi), \
    "+v"(Z.b[2].lo), "+v"(Z.b[2].hi), "+v"(Z.b[3].lo), "+v"(Z.b[3].hi) :: "memory")
  f32x4 acc[4][4];
#pragma unroll
  for (int m = 0; m < 4; ++m)
#pragma unroll
    for (int n = 0; n < 4; ++n) acc[m][n] = (f32x4){0.f, 0.f, 0.f, 0.f};
  auto mma = [&](Fr& Z) {
#pragma unroll
    for (int m = 0; m < 4; ++m)
#pragma unroll
      for (int n = 0; n < 4; ++n) acc[m][n] = MFMA16(FRAG(Z.b[n]), Z.a[m], acc[m][n]); };
  WAIT_V(12); WAIT_L(0); BAR;
  STG_A(4); STG_B(4);
  ldf(F0, IC<0>{});
  const int yoff = ((pn2 * 128 + 8 * wid + (lane >> 3)) * S + pm2 * 128 + kh * 64 + (lane & 7) * 8) * 2;
  const int row0 = pm2 * 128 + kh * 64 + wk * 32 + fr, col0 = pn2 * 128 + wn * 64 + fq * 8;
  u32x4 xv[2][2]; unsigned sg[2];
  auto trip = [&](auto T_) { constexpr int T = T_, SL = (T + 4) % 5, SD = (T + 3) % 5;
    Fr& C = (T & 1) ? F1 : F0; Fr& N = (T & 1) ? F0 : F1;
    constexpr int NV = T == 0 ? 63 : T < 3 ? 16 : T < 25 ? 12 : T == 25 ? 6 : 63;
    asm volatile("s_waitcnt vmcnt(%0)" :: "i"(NV) : "memory");
    WAIT_F(C); if constexpr (T < 26) BAR;
    if constexpr (T == 3) { if (tid == 0) st_agent(ctl + CT_Y + blockIdx.x, tag); }
    if constexpr (T == 24) { if (wm != kh) __builtin_amdgcn_s_setprio(2); }
    if constexpr (T == 24) { if (lane == 0) *(LAS unsigned*)(lds + G2_ST + 16384 + wid * 4) = 0u; }
    if constexpr (T == 25) { if (!__all(*(const LAS u64*)(ldw + G2_ST + (lane & 31) * 8) == tag)) poll_flags<1>(ctl + CT_Y + pn2 * 32 + (lane & 31), tag); }
    auto dma = [&](auto P_) { constexpr int P = P_;
      if constexpr (T < 23 && P < 2) BLDS_X(rH, ldw + SD * G2_ST + P * 8192, voffA, (T + 5) * 2048 + P * 4 * (F / 32) * 1024);
      if constexpr (T < 23 && P >= 2) BLDS(rW, ldw + SD * G2_ST + P * 8192, voffW, (T + 5) * (64 * D * 2) + (P - 2) * 32 * D * 2);
      if constexpr (T == 23 && P == 0) BLDS_X(RSRC(ctl + CT_Y + pn2 * 32, 256), ldw + G2_ST, lane * 16, 0);
      if constexpr ((T == 23 || T == 24) && P > 0) {
        if constexpr (P < 3) xv[T - 23][P - 1] = *(const u32x4*)(X + (size_t)(row0 + 16 * (T - 23)) * D + col0 + 32 * (P - 1));
        else sg[T - 23] = __hip_atomic_load((const unsigned*)(ctl + CT_S + row0 + 16 * (T - 23)), __ATOMIC_RELAXED, __HIP_MEMORY_SCOPE_AGENT); }
      if constexpr (T == 25 && P < 2) BLDS_X(RSRC(y2T, S * D * 2), ldw + G2_ST + P * 8192, yoff, P * 64 * S * 2); };
    auto rd = [&](auto R_) { constexpr int R = R_;
      if constexpr (R < 8) trrd<(SL & 1) * G2_ST>((R & 1) ? N.b[R >> 1].hi : N.b[R >> 1].lo, (R & 1) ? boff[SL >> 1][R >> 1] + hsw : boff[SL >> 1][R >> 1]);
      else rd128<(SL & 1) * G2_ST + (R & 1) * 2048>(N.a[R - 8], aoff[SL >> 1] ^ ((R - 8) >> 1) * 4096); };
    static_for<16>([&](auto I_) { constexpr int I = I_;
      acc[I >> 2][I & 3] = MFMA16(FRAG(C.b[I & 3]), C.a[I >> 2], acc[I >> 2][I & 3]);
      if constexpr (I < 6) { rd(IC<2 * I>{}); rd(IC<2 * I + 1>{}); }
      else if constexpr (I < 14 && !(I & 1)) dma(IC<(I - 6) / 2>{});
      SCHED; }); };
  static_for<27>(trip);
  WAIT_F(F1); mma(F1);
  f32x4 c[2][4];
  LAS f32x4* ro = (LAS f32x4*)(lds + 2 * G2_ST + wid * 8192) + lane; LAS f32x4* ri = (LAS f32x4*)(lds + 2 * G2_ST + (wid ^ 2) * 8192) + lane;
#pragma unroll
  for (int i = 0; i < 8; ++i) ro[i * 64] = acc[2 + (i >> 2)][i & 3];
  const auto rX = RSRC(xbuf, 256 * 32768);
  if (wm != kh) {
    WAIT_L(0);
    if (lane == 0) __hip_atomic_store((LAS unsigned*)(lds + G2_ST + 16384 + wid * 4), 1u, __ATOMIC_RELAXED, __HIP_MEMORY_SCOPE_WORKGROUP);
    for (int spin = 0; spin < (1 << 22) && __hip_atomic_load((LAS unsigned*)(lds + G2_ST + 16384 + (wid ^ 2) * 4), __ATOMIC_RELAXED, __HIP_MEMORY_SCOPE_WORKGROUP) == 0u; ++spin) {}
    asm volatile("" ::: "memory");
#pragma unroll
    for (int i = 0; i < 8; ++i) { const f32x4 t = acc[i >> 2][i & 3] + ri[i * 64];
      __builtin_amdgcn_raw_buffer_store_b128((u32x4){pk2(t[0], t[1]), (unsigned)tag, pk2(t[2], t[3]), (unsigned)tag}, rX, blockIdx.x * 32768 + (wid & 3) * 8192 + i * 1024 + lane * 16, 0, 16); }
    WAIT_V(8); BAR;
  } else {
    WAIT_V(0); WAIT_L(0); BAR;
    asm volatile("" ::: "memory");
#pragma unroll
    for (int i = 0; i < 8; ++i) c[i >> 2][i & 3] = acc[i >> 2][i & 3] + ri[i * 64];
    const bool odd = fq & 1;
    const int qq = fr >> 2, p = lane & 3;
    float r2[2];
#pragma unroll
    for (int i = 0; i < 2; ++i) {
      const float mu = lo16(sg[i]), ri = __builtin_amdgcn_rcpf(hi16(sg[i]));
      r2[i] = ri * ri;
#pragma unroll
      for (int n = 0; n < 4; ++n) {
        LAS unsigned char* yp = lds + G2_ST + (wn * 64 + (n >> 1) * 32 + fq * 8 + (((n ^ fq) & 1) << 2) + qq) * 128 + (wk * 32 + i * 16 + 4 * p) * 2;
        const u32x2 y = __builtin_bit_cast(u32x2, __builtin_amdgcn_ds_read_tr16_b64_v4i16((LAS s16x4*)yp));
        const u32x4 xq = xv[i][n >> 1];
        const unsigned x0 = (odd != (bool)(n & 1)) ? xq[2] : xq[0], x1 = (odd != (bool)(n & 1)) ? xq[3] : xq[1];
        const f32x4 x = {lo16(x0), hi16(x0), lo16(x1), hi16(x1)};
        c[i][n] = x + (x - mu) * ri * (f32x4){lo16(y[0]), hi16(y[0]), lo16(y[1]), hi16(y[1])} + c[i][n] * r2[i];
        asm volatile("" : "+v"(c[i][n]));
      }
    }
    u32x4 g[8];
    const int go = (blockIdx.x ^ 1) * 32768 + (wid & 3) * 8192 + lane * 16;
    for (int spin = 0; spin < (1 << 20); ++spin) {
      bool ok = true;
#pragma unroll
      for (int i = 0; i < 8; ++i) g[i] = __builtin_amdgcn_raw_buffer_load_b128(rX, go + i * 1024, 0, 16);
#pragma unroll
      for (int i = 0; i < 8; ++i) ok &= g[i][1] == (unsigned)tag && g[i][3] == (unsigned)tag;
      if (__all(ok)) break;
      if (!__all(g[0][1] == (unsigned)tag))
        for (int s2 = 0; s2 < (1 << 16); ++s2) {
          const u32x4 t = __builtin_amdgcn_raw_buffer_load_b128(rX, go, 0, 16);
          if (__all(t[1] == (unsigned)tag)) break;
          asm volatile("s_sleep 1" ::: "memory");
        }
      asm volatile("" ::: "memory");
    }
#pragma unroll
    for (int i = 0; i < 2; ++i)
#pragma unroll
      for (int h = 0; h < 2; ++h) {
        u32x2 w[2];
#pragma unroll
        for (int n = 0; n < 2; ++n) { const u32x4 t = g[i * 4 + h * 2 + n];
          const f32x4 o = c[i][h * 2 + n] + (f32x4){lo16(t[0]), hi16(t[0]), lo16(t[2]), hi16(t[2])} * r2[i]; w[n] = (u32x2){pk2(o[0], o[1]), pk2(o[2], o[3])}; }
        __builtin_amdgcn_raw_buffer_store_b128((u32x4){odd ? w[1][0] : w[0][0], odd ? w[1][1] : w[0][1], odd ? w[0][0] : w[1][0], odd ? w[0][1] : w[1][1]},
                                               RSRC(out, S * D * 2), ((row0 + 16 * i) * D + col0 + 32 * h) * 2, 0, 16);
      }
  }
  }
  if (blockIdx.x == 0 && wid == 4) {
    poll_flags<4>(ctl + CT_Y + lane, tag);
    if (lane == 0) st_agent(ctl, tag);
  }
#undef BLDS_X
#undef STG_A
#undef STG_B
#undef WAIT_F
}

extern "C" void kernel_launch(void* const* d_in, const int* in_sizes, int n_in, void* d_out, int out_size, void* d_ws, size_t ws_size, hipStream_t stream) {
  const u16* q = (const u16*)d_in[0]; const u16* k = (const u16*)d_in[1]; const u16* v = (const u16*)d_in[2];
  const u16* shk = (const u16*)d_in[3]; const u16* hk = (const u16*)d_in[4]; const u16* sD = (const u16*)d_in[5];
  const u16* Wg = (const u16*)d_in[7]; const u16* Wu = (const u16*)d_in[8]; const u16* Wd = (const u16*)d_in[9];
  char* ws = (char*)d_ws;
  u64* ctl = (u64*)ws; u16* kvT = (u16*)(ws + (1 << 20)); u16* y2T = (u16*)(ws + (5 << 20)); u16* H = (u16*)(ws + (9 << 20)); float* xbuf = (float*)(ws + (24 << 20));
  (void)hipFuncSetAttribute((const void*)k_fused, hipFuncAttributeMaxDynamicSharedMemorySize, LDS_BYTES);
  k_fused<<<256, 512, LDS_BYTES, stream>>>(q, Wg, Wu, H, ctl, k, v, shk, hk, sD, Wd, kvT, y2T, xbuf, (u16*)d_out);
}
```
